```python
import torch

def _softmax(x, axis=-1):
    return torch.softmax(x, dim=axis)

def _gelu(x):
    return torch.nn.functional.gelu(x, approximate='tanh')

def _silu(x):
    return torch.nn.functional.silu(x)

def _sigmoid(x):
    return torch.sigmoid(x)

def _layernorm(x, axis=-1, eps=1e-05):
    m = x.mean(dim=axis, keepdim=True)
    v = x.var(dim=axis, keepdim=True, unbiased=False)
    return (x - m) / torch.sqrt(v + eps)

def _scatter_add(tgt, idx, src, axis=0):
    return tgt.clone().index_add_(axis, idx, src)

def _segment_sum(src, seg, n):
    out = torch.zeros((n,) + src.shape[1:], dtype=src.dtype)
    return out.index_add_(0, seg, src)

def _pad(x, pw):
    flat = tuple((v for pair in reversed(pw) for v in pair))
    return torch.nn.functional.pad(x, flat)
kernel_name = 'adaLN_cond__memory_aug_recurrent_mem_tr__glu_gated_swiglu__prompt'

def setup_inputs():

    _w = lambda a: (lambda t: t.to(torch.bfloat16) if t.is_floating_point() else t)(torch.as_tensor(a))

    def _A_setup():
        B, S, Dm = (1, 2048, 640)
        H, Dh = (32, 192)
        M, seg = (8, 32)
        seg = min(seg, S)
        S = S // seg * seg
        return {'x': _w((lambda *s: torch.randn(*s, dtype=torch.bfloat16) * 0.05)(B, S, Dm)), 'mem0': _w((lambda *s: torch.randn(*s, dtype=torch.bfloat16) * 0.8)(B, M, Dm)), 'Wq': _w((lambda *s: torch.randn(*s, dtype=torch.bfloat16) * 0.8)(Dm, H, Dh) * Dm ** (-0.5)), 'Wk': _w((lambda *s: torch.randn(*s, dtype=torch.bfloat16) * 0.8)(Dm, H, Dh) * Dm ** (-0.5)), 'Wv': _w((lambda *s: torch.randn(*s, dtype=torch.bfloat16) * 0.8)(Dm, H, Dh) * Dm ** (-0.5)), 'Wo': _w((lambda *s: torch.randn(*s, dtype=torch.bfloat16) * 0.8)(H, Dh, Dm) * (H * Dh) ** (-0.5)), 'seg_len': seg}
    _A_in = _A_setup()

    def _B_setup():
        B, S, D, F = (1, 2048, 640, 1536)
        return {'x': _w((lambda *s: torch.randn(*s, dtype=torch.bfloat16) * 0.05)(B, S, D)), 'w1': _w((lambda *s: torch.randn(*s, dtype=torch.bfloat16) * 0.4)(D, F) * (2.0 / D) ** 0.5), 'w3': _w((lambda *s: torch.randn(*s, dtype=torch.bfloat16) * 0.4)(D, F) * (2.0 / D) ** 0.5), 'w2': _w((lambda *s: torch.randn(*s, dtype=torch.bfloat16) * 0.4)(F, D) * (2.0 / F) ** 0.5)}
    _B_in = _B_setup()
    return {'A_x': _A_in['x'], 'A_mem0': _A_in['mem0'], 'A_Wq': _A_in['Wq'], 'A_Wk': _A_in['Wk'], 'A_Wv': _A_in['Wv'], 'A_Wo': _A_in['Wo'], 'A_seg_len': _A_in['seg_len'], 'B_x': _B_in['x'], 'B_w1': _B_in['w1'], 'B_w3': _B_in['w3'], 'B_w2': _B_in['w2'], 'c': _w((lambda *s: torch.randn(*s, dtype=torch.bfloat16) * 0.1)(1, 640) * 8.0), 'w_mod': _w((lambda *s: torch.randn(*s, dtype=torch.bfloat16) * 0.1)(640, 6 * 640) * 640 ** (-0.5) * 8.0)}

def reference(A_x, A_mem0, A_Wq, A_Wk, A_Wv, A_Wo, A_seg_len, B_x, B_w1, B_w3, B_w2, c, w_mod):

    def _A_ref(x, mem0, Wq, Wk, Wv, Wo, seg_len):
        B, S, Dm = x.shape
        H, Dh = (Wq.shape[1], Wq.shape[2])
        M = mem0.shape[1]
        scale = Dh ** (-0.5)
        n_seg = S // seg_len
        L = M + seg_len
        idx = torch.arange(L)
        mask = (idx[:, None] < M) | (idx[:, None] >= idx[None, :])
        mem = mem0
        outs = []
        for s in range(n_seg):
            xs = x[:, s * seg_len:(s + 1) * seg_len, :].clone()
            h = torch.concatenate([mem, xs], axis=1)
            q = torch.einsum('btd,dhe->bhte', h, Wq)
            k = torch.einsum('btd,dhe->bhte', h, Wk)
            v = torch.einsum('btd,dhe->bhte', h, Wv)
            scores = torch.einsum('bhqe,bhke->bhqk', q, k) * scale
            scores = torch.where(mask[None, None], scores, -1000000000.0)
            p = _softmax(scores, axis=-1)
            o = torch.einsum('bhqk,bhke->bhqe', p, v)
            o = torch.einsum('bhte,hed->btd', o, Wo)
            mem = o[:, :M, :].clone()
            outs.append(o[:, M:, :])
        return torch.concatenate(outs, axis=1)

    def _B_ref(x, w1, w3, w2):
        gate = torch.einsum('bsd,df->bsf', x, w1)
        up = torch.einsum('bsd,df->bsf', x, w3)
        h = _silu(gate) * up
        out = torch.einsum('bsf,fd->bsd', h, w2)
        return out
    x = A_x
    mod = (_silu(c) @ w_mod).reshape(c.shape[0], 6, 1, -1)
    sa, ca, ga, sb, cb, gb = (mod[:, 0].clone(), mod[:, 1].clone(), mod[:, 2].clone(), mod[:, 3].clone(), mod[:, 4].clone(), mod[:, 5].clone())
    h = _layernorm(x, axis=-1) * (1 + ca) + sa
    x = x + ga * _A_ref(h, A_mem0, A_Wq, A_Wk, A_Wv, A_Wo, A_seg_len)
    h = _layernorm(x, axis=-1) * (1 + cb) + sb
    x = x + gb * _B_ref(h, B_w1, B_w3, B_w2)
    return x
```

```cpp
#include <hip/hip_runtime.h>
#include <hip/hip_bf16.h>
#include <stdint.h>

#define LAS __attribute__((address_space(3)))
#define GAS __attribute__((address_space(1)))
typedef short bf16x8 __attribute__((ext_vector_type(8)));
typedef short bf16x4 __attribute__((ext_vector_type(4)));
typedef float f32x4 __attribute__((ext_vector_type(4)));
typedef unsigned v4u __attribute__((ext_vector_type(4)));
typedef unsigned v2u __attribute__((ext_vector_type(2)));
typedef unsigned short u16;
typedef GAS unsigned gu32;
#define RLX_AGENT __ATOMIC_RELAXED, __HIP_MEMORY_SCOPE_AGENT

constexpr int S_ = 2048, D_ = 640, H_ = 32, E_ = 192, HE = 6144, NSEG = 64, F_ = 1536;
constexpr size_t OFF_MOD = 0;
constexpr size_t OFF_CTL = 16384;
constexpr size_t OFF_H   = 32768;
constexpr size_t OFF_Q   = OFF_H + (size_t)S_ * D_ * 2;
constexpr size_t OFF_K   = OFF_Q + (size_t)S_ * HE * 2;
constexpr size_t OFF_UX  = OFF_K + (size_t)S_ * HE * 2;
constexpr size_t OFF_MEM = OFF_UX + (size_t)NSEG * H_ * 40 * 4 * 256;
constexpr size_t OFF_P   = OFF_MEM + (size_t)65 * 10240;
constexpr size_t OFF_UM  = OFF_P + (size_t)NSEG * 20480;
constexpr size_t OFF_KM  = OFF_UM + (size_t)NSEG * H_ * 40 * 256;
constexpr size_t OFF_PT  = OFF_KM + (size_t)NSEG * H_ * 8 * E_ * 2;
constexpr size_t OFF_X1  = OFF_PT + (size_t)S_ * 1280 * 2;
constexpr size_t OFF_H2  = OFF_X1 + (size_t)S_ * D_ * 2;
constexpr size_t OFF_ACT = OFF_H2 + (size_t)S_ * D_ * 2;
constexpr size_t OFF_END = OFF_ACT + (size_t)S_ * F_ * 2;
constexpr unsigned SENT = 0xFFFFFFFFu;
constexpr unsigned kSpinCap = 1u << 20;

__device__ __forceinline__ float bf2f(u16 v) { return __uint_as_float((unsigned)v << 16); }
__device__ __forceinline__ float bflo(unsigned v) { return __uint_as_float(v << 16); }
__device__ __forceinline__ float bfhi(unsigned v) { return __uint_as_float(v & 0xFFFF0000u); }
typedef __bf16 nbf2 __attribute__((ext_vector_type(2)));
__device__ __forceinline__ unsigned pk2(float a, float b) { nbf2 t = {(__bf16)a, (__bf16)b}; return __builtin_bit_cast(unsigned, t); }
__device__ __forceinline__ float rbf(float a) { return bflo(pk2(a, 0.f)); }
__device__ __forceinline__ f32x4 mfma16(bf16x8 a, bf16x8 b, f32x4 c) { return __builtin_amdgcn_mfma_f32_16x16x32_bf16(a, b, c, 0, 0, 0); }
__device__ __forceinline__ bf16x4 trread(const char* lds_base, int off) { return __builtin_amdgcn_ds_read_tr16_b64_v4i16((LAS bf16x4*)(lds_base + off)); }
__device__ __forceinline__ bf16x8 cat8(bf16x4 a, bf16x4 b) { return __builtin_shufflevector(a, b, 0, 1, 2, 3, 4, 5, 6, 7); }
__device__ __forceinline__ v4u ld16c(__amdgpu_buffer_rsrc_t r, unsigned off) { return __builtin_amdgcn_raw_buffer_load_b128(r, off, 0, 16); }
__device__ __forceinline__ void st16c(__amdgpu_buffer_rsrc_t r, unsigned off, v4u v) { __builtin_amdgcn_raw_buffer_store_b128(v, r, off, 0, 16); }
__device__ __forceinline__ void st8c(__amdgpu_buffer_rsrc_t r, unsigned off, v2u v) { __builtin_amdgcn_raw_buffer_store_b64(v, r, off, 0, 16); }
__device__ __forceinline__ void st4c(__amdgpu_buffer_rsrc_t r, unsigned off, unsigned v) { __builtin_amdgcn_raw_buffer_store_b32(v, r, off, 0, 16); }
__device__ __forceinline__ bool okv(v4u v) { return v.x != SENT && v.y != SENT && v.z != SENT && v.w != SENT; }

__device__ __forceinline__ int boff192(int row, int ch) { int f = ((row >> 1) & 1) | (((row >> 3) & 1) << 1); return row * 384 + (((ch >> 1) ^ f) << 5) + ((ch & 1) << 4); }
template <int RB> __device__ __forceinline__ int boffP(int row, int ch) { int f = (row & 3) | (((row >> 3) & 1) << 2); return row * RB + (((ch >> 1) ^ f) << 5) + ((ch & 1) << 4); }

__device__ __forceinline__ bf16x8 trfrag192(const char* base, int r0, int c, int lane) {
    const int g = lane >> 4, q = (lane & 15) >> 2, p = lane & 3;
    const int f = ((q >> 1) & 1) | ((g & 1) << 1);
    const int a = (r0 + 8 * g + q) * 384 + ((c ^ f) << 5) + 8 * p;
    return cat8(trread(base, a), trread(base, a + 4 * 384));
}
template <int RB> __device__ __forceinline__ bf16x8 trfragP(const char* base, int r0, int c, int lane) {
    const int g = lane >> 4, q = (lane & 15) >> 2, p = lane & 3;
    const int f = q | ((g & 1) << 2);
    const int a = (r0 + 8 * g + q) * RB + ((c ^ f) << 5) + 8 * p;
    return cat8(trread(base, a), trread(base, a + 4 * RB));
}

__global__ void __launch_bounds__(256) k_pre(const u16* __restrict__ c, const u16* __restrict__ wmod, const u16* __restrict__ mem0, char* ws) {
    __shared__ float sc[640]; __shared__ float red[4][64];
    const int b = blockIdx.x, tid = threadIdx.x;
    if (b < 60) {
        for (int i = tid; i < 640; i += 256) { float v = bf2f(c[i]); sc[i] = rbf(v / (1.f + __expf(-v))); }
        __syncthreads();
        const int col = b * 64 + (tid & 63), kc = tid >> 6; float a = 0.f;
        for (int k = kc * 160; k < kc * 160 + 160; ++k) a += sc[k] * bf2f(wmod[(size_t)k * 3840 + col]);
        red[kc][tid & 63] = a; __syncthreads();
        if (tid < 64) ((float*)(ws + OFF_MOD))[col] = rbf(red[0][tid] + red[1][tid] + red[2][tid] + red[3][tid]);
    } else if (b == 60) {
        for (int ci = tid; ci < 640; ci += 256) { int i = ci & 7, kg = ci >> 3; *(v4u*)(ws + OFF_MEM + ci * 16) = *(const v4u*)(mem0 + i * 640 + kg * 8); }
        if (tid < 64) ((unsigned*)(ws + OFF_CTL))[tid] = 0;
    }
    const size_t beg = OFF_MEM + 10240, n16 = (OFF_KM - beg) / 16;
    v4u sv = {SENT, SENT, SENT, SENT};
    for (size_t i = (size_t)b * 256 + tid; i < n16; i += (size_t)gridDim.x * 256) *(v4u*)(ws + beg + i * 16) = sv;
}

__global__ void __launch_bounds__(256) k_ln(const u16* __restrict__ x, const float* __restrict__ mod, int so, int co, u16* __restrict__ out) {
    const int row = blockIdx.x * 4 + (threadIdx.x >> 6), lane = threadIdx.x & 63;
    const unsigned* xr = (const unsigned*)(x + (size_t)row * D_);
    float v[10]; float s = 0.f;
#pragma unroll
    for (int j = 0; j < 5; ++j) { unsigned u = xr[j * 64 + lane]; v[2 * j] = bflo(u); v[2 * j + 1] = bfhi(u); s += v[2 * j] + v[2 * j + 1]; }
#pragma unroll
    for (int m = 32; m >= 1; m >>= 1) s += __shfl_xor(s, m);
    const float mean = s * (1.f / D_); float q = 0.f;
#pragma unroll
    for (int j = 0; j < 10; ++j) { float d = v[j] - mean; q += d * d; }
#pragma unroll
    for (int m = 32; m >= 1; m >>= 1) q += __shfl_xor(q, m);
    const float rstd = 1.f / sqrtf(q * (1.f / D_) + 1e-5f);
    unsigned* orow = (unsigned*)(out + (size_t)row * D_);
#pragma unroll
    for (int j = 0; j < 5; ++j) { int c0 = (j * 64 + lane) * 2;
        float a = (v[2 * j] - mean) * rstd * (1.f + mod[co + c0]) + mod[so + c0];
        float b = (v[2 * j + 1] - mean) * rstd * (1.f + mod[co + c0 + 1]) + mod[so + c0 + 1];
        orow[j * 64 + lane] = pk2(a, b); }
}

constexpr int GA_B = 128 * 128;
constexpr int GB_B = 64 * 384;
constexpr int GBUF = GA_B + GB_B;
template <class BPtr>
__device__ __forceinline__ void gemm128x192(char* lds, const u16* __restrict__ A, int lda, int K, BPtr bptr, f32x4 (&acc)[2][6]) {
    const int tid = threadIdx.x, lane = tid & 63, w = tid >> 6, wm = w >> 1, wn = w & 1, g = lane >> 4, l15 = lane & 15;
#pragma unroll
    for (int a = 0; a < 2; ++a)
#pragma unroll
        for (int b = 0; b < 6; ++b) acc[a][b] = (f32x4){0.f, 0.f, 0.f, 0.f};
    v4u ra[2], rb[3];
    auto gload = [&](int kc) {
#pragma unroll
        for (int i = 0; i < 2; ++i) { int idx = tid + 512 * i, r = idx >> 3, ch = idx & 7; ra[i] = *(const v4u*)(A + (size_t)r * lda + kc * 64 + ch * 8); }
#pragma unroll
        for (int i = 0; i < 3; ++i) { int idx = tid + 512 * i, r = idx / 24, ch = idx % 24; rb[i] = *(const v4u*)bptr(kc * 64 + r, ch); }
    };
    auto lstore = [&](char* buf) {
#pragma unroll
        for (int i = 0; i < 2; ++i) { int idx = tid + 512 * i, r = idx >> 3, ch = idx & 7; *(v4u*)(buf + r * 128 + ((ch ^ (r & 7)) << 4)) = ra[i]; }
#pragma unroll
        for (int i = 0; i < 3; ++i) { int idx = tid + 512 * i, r = idx / 24, ch = idx % 24; *(v4u*)(buf + GA_B + boff192(r, ch)) = rb[i]; }
    };
    const int nk = K / 64;
    gload(0); lstore(lds); __syncthreads();
    for (int kc = 0; kc < nk; ++kc) {
        char* cur = lds + (kc & 1) * GBUF; char* nxt = lds + ((kc + 1) & 1) * GBUF;
        if (kc + 1 < nk) gload(kc + 1);
#pragma unroll
        for (int ks = 0; ks < 2; ++ks) {
            bf16x8 af[2], bfr[6];
#pragma unroll
            for (int mt = 0; mt < 2; ++mt) { int r = wm * 32 + mt * 16 + l15; af[mt] = *(const bf16x8*)(cur + r * 128 + (((4 * ks + g) ^ (r & 7)) << 4)); }
#pragma unroll
            for (int nt = 0; nt < 6; ++nt) bfr[nt] = trfrag192(cur + GA_B, 32 * ks, wn * 6 + nt, lane);
#pragma unroll
            for (int mt = 0; mt < 2; ++mt)
#pragma unroll
                for (int nt = 0; nt < 6; ++nt) acc[mt][nt] = mfma16(bfr[nt], af[mt], acc[mt][nt]);
        }
        if (kc + 1 < nk) lstore(nxt);
        __syncthreads();
    }
}

constexpr int KA_VOFF = 2 * GBUF;
constexpr int KA_VSTR = 400;
constexpr int KA_LDS = KA_VOFF + 128 * KA_VSTR;
__device__ __forceinline__ void taskA(char* lds, int rbk, int h, const u16* __restrict__ Hb, const u16* __restrict__ Wq, const u16* __restrict__ Wk, const u16* __restrict__ Wv,
                                      const u16* __restrict__ Wo, char* ws) {
    const int tid = threadIdx.x, lane = tid & 63, w = tid >> 6, wm = w >> 1, wn = w & 1, g = lane >> 4, l15 = lane & 15;
    const u16* A = Hb + (size_t)rbk * 128 * D_;
    f32x4 acc[2][6];
#pragma unroll 1
    for (int mat = 0; mat < 3; ++mat) {
        const u16* W = (mat == 0 ? Wk : (mat == 1 ? Wq : Wv)) + h * E_;
        gemm128x192(lds, A, D_, D_, [&](int k, int ch) { return W + (size_t)k * HE + ch * 8; }, acc);
        if (mat < 2) {
            u16* O = (u16*)(ws + (mat == 0 ? OFF_K : OFF_Q));
#pragma unroll
            for (int mt = 0; mt < 2; ++mt)
#pragma unroll
                for (int nt = 0; nt < 6; ++nt) {
                    int tok = rbk * 128 + wm * 32 + mt * 16 + l15, e = wn * 96 + nt * 16 + 4 * g;
                    *(v2u*)(O + (size_t)tok * HE + h * E_ + e) = (v2u){pk2(acc[mt][nt][0], acc[mt][nt][1]), pk2(acc[mt][nt][2], acc[mt][nt][3])};
                }
        } else {
#pragma unroll
            for (int mt = 0; mt < 2; ++mt)
#pragma unroll
                for (int nt = 0; nt < 6; ++nt) {
                    int tok = wm * 32 + mt * 16 + l15, e = wn * 96 + nt * 16 + 4 * g;
                    *(v2u*)(lds + KA_VOFF + tok * KA_VSTR + e * 2) = (v2u){pk2(acc[mt][nt][0], acc[mt][nt][1]), pk2(acc[mt][nt][2], acc[mt][nt][3])};
                }
        }
    }
    __syncthreads();
    bf16x8 vf[2][6];
#pragma unroll
    for (int mt = 0; mt < 2; ++mt)
#pragma unroll
        for (int t = 0; t < 6; ++t) vf[mt][t] = *(const bf16x8*)(lds + KA_VOFF + (wm * 32 + mt * 16 + l15) * KA_VSTR + (4 * t + g) * 16);
    const u16* Woh = Wo + (size_t)h * E_ * D_;
    const int seg = rbk * 4 + wm;
    v4u rb[6];
    auto gl = [&](int nc) {
#pragma unroll
        for (int i = 0; i < 6; ++i) { int idx = tid + 512 * i, r = idx >> 4, ch = idx & 15; rb[i] = *(const v4u*)(Woh + (size_t)r * D_ + nc * 128 + ch * 8); }
    };
    gl(0);
#pragma unroll 1
    for (int nc = 0; nc < 5; ++nc) {
        char* buf = lds + (nc & 1) * 49152;
        buf = lds + (nc & 1) * 0;
        __syncthreads();
#pragma unroll
        for (int i = 0; i < 6; ++i) { int idx = tid + 512 * i, r = idx >> 4, ch = idx & 15; *(v4u*)(buf + boffP<256>(r, ch)) = rb[i]; }
        __syncthreads();
        if (nc + 1 < 5) gl(nc + 1);
        f32x4 ua[2][4];
#pragma unroll
        for (int mt = 0; mt < 2; ++mt)
#pragma unroll
            for (int nt = 0; nt < 4; ++nt) ua[mt][nt] = (f32x4){0.f, 0.f, 0.f, 0.f};
#pragma unroll
        for (int t = 0; t < 6; ++t) {
            bf16x8 wf[4];
#pragma unroll
            for (int nt = 0; nt < 4; ++nt) wf[nt] = trfragP<256>(buf, 32 * t, wn * 4 + nt, lane);
#pragma unroll
            for (int mt = 0; mt < 2; ++mt)
#pragma unroll
                for (int nt = 0; nt < 4; ++nt) ua[mt][nt] = mfma16(vf[mt][t], wf[nt], ua[mt][nt]);
        }
#pragma unroll
        for (int mt = 0; mt < 2; ++mt)
#pragma unroll
            for (int nt = 0; nt < 4; ++nt) {
                int r = nc * 8 + wn * 4 + nt, jg = 2 * mt + (g >> 1);
                size_t off = OFF_UX + ((((size_t)(seg * 32 + h) * 40 + r) * 4 + jg) * 16 + l15) * 16 + (g & 1) * 8;
                *(v2u*)(ws + off) = (v2u){pk2(ua[mt][nt][0], ua[mt][nt][1]), pk2(ua[mt][nt][2], ua[mt][nt][3])};
            }
    }
}
__global__ void __launch_bounds__(512, 2) kA(const u16* Wq, const u16* Wk, const u16* Wv, const u16* Wo, char* ws) {
    extern __shared__ __attribute__((aligned(16))) char lds[];
    const int b = blockIdx.x;
    taskA(lds, b >> 5, b & 31, (const u16*)(ws + OFF_H), Wq, Wk, Wv, Wo, ws);
}

constexpr int C_RES = 0;
constexpr int C_MEM = 122880;
constexpr int C_KX  = 133120;
constexpr int C_QK  = 145920;
constexpr int C_FLAG = 158720;
constexpr int C_LDS = 158720 + 512 + 4096;

template <int N> __device__ __forceinline__ bool poll_copy(__amdgpu_buffer_rsrc_t rs, unsigned goff, char* ldst, int nchunks, int tid) {
    v4u v[N]; unsigned spins = 0; bool dead = false;
    while (true) {
        asm volatile("" ::: "memory");
        bool ok = true;
#pragma unroll
        for (int k = 0; k < N; ++k) { int i = tid + k * 512; if (i < nchunks) v[k] = ld16c(rs, goff + i * 16); }
#pragma unroll
        for (int k = 0; k < N; ++k) { int i = tid + k * 512; if (i < nchunks) ok &= okv(v[k]); }
        if (ok) break;
        if (++spins > kSpinCap) { dead = true; break; }
    }
#pragma unroll
    for (int k = 0; k < N; ++k) { int i = tid + k * 512; if (i < nchunks) *(v4u*)(ldst + i * 16) = v[k]; }
    return dead;
}

__device__ __forceinline__ void chain_qk(char* lds, int h, const u16* __restrict__ Wq, const u16* __restrict__ Wk, char* ws, __amdgpu_buffer_rsrc_t rs) {
    const int tid = threadIdx.x, lane = tid & 63, w = tid >> 6, g = lane >> 4, l15 = lane & 15;
    const int isk = w >> 2;
    const int et0 = 3 * (w & 3);
    bf16x8 wr[3][15];
#pragma unroll 1
    for (int c = 0; c < 5; ++c) {
        __syncthreads();
        for (int idx = tid; idx < 2 * 96 * 24; idx += 512) {
            int m = idx / (96 * 24), rem = idx % (96 * 24), r = rem / 24, ch = rem % 24;
            const u16* W = (m ? Wk : Wq) + (size_t)(c * 96 + r) * HE + h * E_ + ch * 8;
            *(v4u*)(lds + m * 36864 + boff192(r, ch)) = *(const v4u*)W;
        }
        __syncthreads();
#pragma unroll
        for (int cc = 0; cc < 5; ++cc) if (cc == c) {
#pragma unroll
            for (int k3 = 0; k3 < 3; ++k3)
#pragma unroll
                for (int j = 0; j < 3; ++j) wr[j][cc * 3 + k3] = trfrag192(lds + isk * 36864, 32 * k3, et0 + j, lane);
        }
    }
    __syncthreads();
    for (int idx = tid; idx < 2 * 160 * 24; idx += 512) {
        int m = idx / (160 * 24), rem = idx % (160 * 24), r = rem / 24, ch = rem % 24;
        const u16* W = (m ? Wk : Wq) + (size_t)(480 + r) * HE + h * E_ + ch * 8;
        *(v4u*)(lds + C_RES + m * 61440 + boff192(r, ch)) = *(const v4u*)W;
    }
    const u16* Kg = (const u16*)(ws + OFF_K);
    for (int idx = tid; idx < 32 * 24; idx += 512) { int r = idx / 24, ch = idx % 24; *(v4u*)(lds + C_KX + r * 400 + ch * 16) = *(const v4u*)(Kg + (size_t)r * HE + h * E_ + ch * 8); }
    if (tid == 0) *(volatile int*)(lds + C_FLAG) = 0;
    __syncthreads();
    const float scale = 0.07216878364870322f;
#pragma unroll 1
    for (int s = 0; s < NSEG; ++s) {
        v4u kx[2];
        if (s + 1 < NSEG) {
#pragma unroll
            for (int i = 0; i < 2; ++i) { int idx = tid + 512 * i; if (idx < 768) { int r = idx / 24, ch = idx % 24; kx[i] = *(const v4u*)(Kg + (size_t)((s + 1) * 32 + r) * HE + h * E_ + ch * 8); } }
        }
        bool dead = poll_copy<2>(rs, (unsigned)(OFF_MEM - OFF_MEM) + s * 10240, lds + C_MEM, 640, tid);
        if (dead) *(volatile int*)(lds + C_FLAG) = 1;
        __syncthreads();
        if (*(volatile int*)(lds + C_FLAG)) break;
        f32x4 acc[3] = {{0.f, 0.f, 0.f, 0.f}, {0.f, 0.f, 0.f, 0.f}, {0.f, 0.f, 0.f, 0.f}};
#pragma unroll
        for (int ks = 0; ks < 20; ++ks) {
            bf16x8 b = *(const bf16x8*)(lds + C_MEM + ks * 512 + (g * 8 + (lane & 7)) * 16);
#pragma unroll
            for (int j = 0; j < 3; ++j) {
                bf16x8 a = (ks < 15) ? wr[j][ks < 15 ? ks : 0] : trfrag192(lds + C_RES + isk * 61440, 32 * (ks - 15), et0 + j, lane);
                acc[j] = mfma16(a, b, acc[j]);
            }
        }
        if (l15 < 8) {
#pragma unroll
            for (int j = 0; j < 3; ++j) {
                v2u pv = {pk2(acc[j][0], acc[j][1]), pk2(acc[j][2], acc[j][3])};
                int e = 16 * (et0 + j) + 4 * g;
                *(v2u*)(lds + C_QK + (isk * 8 + l15) * 400 + e * 2) = pv;
                if (isk) *(v2u*)(ws + OFF_KM + (((size_t)(s * 32 + h) * 8 + l15) * E_ + e) * 2) = pv;
            }
        }
        __syncthreads();
        if (w == 0 && s + 1 < NSEG) {
            f32x4 sc[3] = {{0.f, 0.f, 0.f, 0.f}, {0.f, 0.f, 0.f, 0.f}, {0.f, 0.f, 0.f, 0.f}};
#pragma unroll
            for (int t = 0; t < 6; ++t) {
                bf16x8 qf = *(const bf16x8*)(lds + C_QK + (lane & 7) * 400 + (4 * t + g) * 16);
                bf16x8 k0 = *(const bf16x8*)(lds + C_QK + (8 + (lane & 7)) * 400 + (4 * t + g) * 16);
                bf16x8 k1 = *(const bf16x8*)(lds + C_KX + l15 * 400 + (4 * t + g) * 16);
                bf16x8 k2 = *(const bf16x8*)(lds + C_KX + (16 + l15) * 400 + (4 * t + g) * 16);
                sc[0] = mfma16(k0, qf, sc[0]); sc[1] = mfma16(k1, qf, sc[1]); sc[2] = mfma16(k2, qf, sc[2]);
            }
            float mx = -3.0e38f;
#pragma unroll
            for (int r = 0; r < 4; ++r) { sc[0][r] = (g < 2) ? rbf(rbf(sc[0][r]) * scale) : -3.0e38f; sc[1][r] = rbf(rbf(sc[1][r]) * scale); sc[2][r] = rbf(rbf(sc[2][r]) * scale);
                mx = fmaxf(mx, fmaxf(sc[0][r], fmaxf(sc[1][r], sc[2][r]))); }
            mx = fmaxf(mx, __shfl_xor(mx, 16)); mx = fmaxf(mx, __shfl_xor(mx, 32));
            float sum = 0.f;
#pragma unroll
            for (int r = 0; r < 4; ++r) { sc[0][r] = (g < 2) ? __expf(sc[0][r] - mx) : 0.f; sc[1][r] = __expf(sc[1][r] - mx); sc[2][r] = __expf(sc[2][r] - mx); sum += sc[0][r] + sc[1][r] + sc[2][r]; }
            sum += __shfl_xor(sum, 16); sum += __shfl_xor(sum, 32);
            const float inv = 1.f / sum;
            if (l15 < 8) {
                const unsigned pb = (unsigned)(OFF_P - OFF_MEM) + s * 20480 + h * 640 + l15 * 16 + (g & 1) * 8;
                if (g < 2) st8c(rs, pb, (v2u){pk2(sc[0][0] * inv, sc[0][1] * inv), pk2(sc[0][2] * inv, sc[0][3] * inv)});
                st8c(rs, pb + (1 + (g >> 1)) * 128, (v2u){pk2(sc[1][0] * inv, sc[1][1] * inv), pk2(sc[1][2] * inv, sc[1][3] * inv)});
                st8c(rs, pb + (3 + (g >> 1)) * 128, (v2u){pk2(sc[2][0] * inv, sc[2][1] * inv), pk2(sc[2][2] * inv, sc[2][3] * inv)});
            }
        }
        __syncthreads();
        if (s + 1 < NSEG) {
#pragma unroll
            for (int i = 0; i < 2; ++i) { int idx = tid + 512 * i; if (idx < 768) { int r = idx / 24, ch = idx % 24; *(v4u*)(lds + C_KX + r * 400 + ch * 16) = kx[i]; } }
        }
    }
}

__device__ __forceinline__ void chain_vo(char* lds, int h, const u16* __restrict__ Wv, const u16* __restrict__ Wo, char* ws, __amdgpu_buffer_rsrc_t rs) {
    const int tid = threadIdx.x, lane = tid & 63, w = tid >> 6, g = lane >> 4, l15 = lane & 15;
    const int kh = w >> 2, et0 = 3 * (w & 3);
    bf16x8 wv[3][10], wo[5][3];
#pragma unroll 1
    for (int c = 0; c < 5; ++c) {
        __syncthreads();
        for (int idx = tid; idx < 2 * 64 * 24; idx += 512) {
            int m = idx / (64 * 24), rem = idx % (64 * 24), r = rem / 24, ch = rem % 24;
            *(v4u*)(lds + m * 24576 + boff192(r, ch)) = *(const v4u*)(Wv + (size_t)(m * 320 + c * 64 + r) * HE + h * E_ + ch * 8);
        }
        __syncthreads();
#pragma unroll
        for (int cc = 0; cc < 5; ++cc) if (cc == c) {
#pragma unroll
            for (int k2 = 0; k2 < 2; ++k2)
#pragma unroll
                for (int j = 0; j < 3; ++j) wv[j][cc * 2 + k2] = trfrag192(lds + kh * 24576, 32 * k2, et0 + j, lane);
        }
    }
    const u16* Woh = Wo + (size_t)h * E_ * D_;
    __syncthreads();
    for (int idx = tid; idx < 96 * 80; idx += 512) { int r = idx / 80, ch = idx % 80; *(v4u*)(lds + boffP<1280>(r, ch)) = *(const v4u*)(Woh + (size_t)r * D_ + ch * 8); }
    __syncthreads();
#pragma unroll
    for (int n = 0; n < 5; ++n)
#pragma unroll
        for (int t = 0; t < 3; ++t) wo[n][t] = trfragP<1280>(lds, 32 * t, 5 * w + n, lane);
    __syncthreads();
    for (int idx = tid; idx < 96 * 80; idx += 512) { int r = idx / 80, ch = idx % 80; *(v4u*)(lds + C_RES + boffP<1280>(r, ch)) = *(const v4u*)(Woh + (size_t)(96 + r) * D_ + ch * 8); }
    if (tid == 0) *(volatile int*)(lds + C_FLAG) = 0;
    __syncthreads();
    float* vpart = (float*)(lds + C_KX);
    char* vfr = lds + C_QK;
#pragma unroll 1
    for (int s = 0; s < NSEG; ++s) {
        bool dead = poll_copy<2>(rs, s * 10240, lds + C_MEM, 640, tid);
        if (dead) *(volatile int*)(lds + C_FLAG) = 1;
        __syncthreads();
        if (*(volatile int*)(lds + C_FLAG)) break;
        f32x4 acc[3] = {{0.f, 0.f, 0.f, 0.f}, {0.f, 0.f, 0.f, 0.f}, {0.f, 0.f, 0.f, 0.f}};
#pragma unroll
        for (int ks = 0; ks < 10; ++ks) {
            bf16x8 b = *(const bf16x8*)(lds + C_MEM + (kh * 10 + ks) * 512 + (g * 8 + (lane & 7)) * 16);
#pragma unroll
            for (int j = 0; j < 3; ++j) acc[j] = mfma16(wv[j][ks], b, acc[j]);
        }
        if (l15 < 8) {
#pragma unroll
            for (int j = 0; j < 3; ++j) *(f32x4*)(vpart + (kh * 8 + l15) * 192 + 16 * (et0 + j) + 4 * g) = acc[j];
        }
        __syncthreads();
        if (tid < 192) {
            int i = tid & 7, kg = tid >> 3;
            const float* p0 = vpart + i * 192 + kg * 8; const float* p1 = p0 + 8 * 192;
            f32x4 a0 = *(const f32x4*)p0, a1 = *(const f32x4*)(p0 + 4), b0 = *(const f32x4*)p1, b1 = *(const f32x4*)(p1 + 4);
            a0 += b0; a1 += b1;
            *(v4u*)(vfr + tid * 16) = (v4u){pk2(a0[0], a0[1]), pk2(a0[2], a0[3]), pk2(a1[0], a1[1]), pk2(a1[2], a1[3])};
        }
        __syncthreads();
        f32x4 ua[5];
#pragma unroll
        for (int n = 0; n < 5; ++n) ua[n] = (f32x4){0.f, 0.f, 0.f, 0.f};
#pragma unroll
        for (int t = 0; t < 6; ++t) {
            bf16x8 a = *(const bf16x8*)(vfr + t * 512 + (g * 8 + (lane & 7)) * 16);
#pragma unroll
            for (int n = 0; n < 5; ++n) {
                bf16x8 b = (t < 3) ? wo[n][t < 3 ? t : 0] : trfragP<1280>(lds + C_RES, 32 * (t - 3), 5 * w + n, lane);
                ua[n] = mfma16(a, b, ua[n]);
            }
        }
        if (g < 2) {
#pragma unroll
            for (int n = 0; n < 5; ++n) {
                unsigned off = (unsigned)(OFF_UM - OFF_MEM) + ((s * 32 + h) * 40 + 5 * w + n) * 256 + l15 * 16 + g * 8;
                st8c(rs, off, (v2u){pk2(ua[n][0], ua[n][1]), pk2(ua[n][2], ua[n][3])});
            }
        }
    }
}

__device__ __forceinline__ void chain_red(char* lds, int r, char* ws, __amdgpu_buffer_rsrc_t rs) {
    const int tid = threadIdx.x, lane = tid & 63, w = tid >> 6, g = lane >> 4, l15 = lane & 15;
    float* red = (float*)lds;
    if (tid == 0) *(volatile int*)(lds + C_FLAG) = 0;
    __syncthreads();
    auto ldux = [&](int s, v4u (&ux)[5]) {
#pragma unroll
        for (int t = 0; t < 5; ++t) { int cgl = 4 * t + g, hh = cgl / 5, jg = cgl % 5; int jj = jg ? jg - 1 : 0;
            ux[t] = *(const v4u*)(ws + OFF_UX + ((((size_t)(s * 32 + 4 * w + hh) * 40 + r) * 4 + jj) * 16 + l15) * 16); }
    };
    v4u ux[5]; ldux(0, ux);
#pragma unroll 1
    for (int s = 0; s + 1 < NSEG; ++s) {
        v4u pf[5], um; unsigned spins = 0; bool dead = false;
        const unsigned pb = (unsigned)(OFF_P - OFF_MEM) + s * 20480 + ((20 * w + g) * 8 + (lane & 7)) * 16;
        const unsigned ub = (unsigned)(OFF_UM - OFF_MEM) + ((s * 32 + 4 * w + g) * 40 + r) * 256 + l15 * 16;
        while (true) {
            asm volatile("" ::: "memory");
#pragma unroll
            for (int t = 0; t < 5; ++t) pf[t] = ld16c(rs, pb + t * 512);
            um = ld16c(rs, ub);
            bool ok = okv(um);
#pragma unroll
            for (int t = 0; t < 5; ++t) ok &= okv(pf[t]);
            if (__all(ok)) break;
            if (++spins > kSpinCap) { dead = true; break; }
        }
        f32x4 acc = {0.f, 0.f, 0.f, 0.f};
#pragma unroll
        for (int t = 0; t < 5; ++t) {
            v4u a = (t < 4 && g == t) ? um : ux[t];
            acc = mfma16(__builtin_bit_cast(bf16x8, a), __builtin_bit_cast(bf16x8, pf[t]), acc);
        }
        if (l15 < 8) *(f32x4*)(red + (w * 8 + l15) * 16 + 4 * g) = acc;
        if (dead) *(volatile int*)(lds + C_FLAG) = 1;
        if (s + 2 < NSEG) ldux(s + 1, ux);
        __syncthreads();
        if (*(volatile int*)(lds + C_FLAG)) break;
        if (w == 0) {
            int i = lane >> 3, cp = lane & 7; float a0 = 0.f, a1 = 0.f;
#pragma unroll
            for (int q = 0; q < 8; ++q) { float2 p = *(const float2*)(red + (q * 8 + i) * 16 + 2 * cp); a0 += p.x; a1 += p.y; }
            st4c(rs, (s + 1) * 10240 + r * 256 + (cp >> 2) * 128 + i * 16 + (cp & 3) * 4, pk2(a0, a1));
        }
        __syncthreads();
    }
}

__global__ void __launch_bounds__(512, 2) k_chain(const u16* Wq, const u16* Wk, const u16* Wv, const u16* Wo, char* ws) {
    extern __shared__ __attribute__((aligned(16))) char lds[];
    const int b = blockIdx.x;
    const __amdgpu_buffer_rsrc_t rs = __builtin_amdgcn_make_buffer_rsrc(ws + OFF_MEM, 0, (int)(OFF_KM - OFF_MEM), 0x00020000);
    if (b < 32) chain_qk(lds, b, Wq, Wk, ws, rs);
    else if (b < 64) chain_vo(lds, b - 32, Wv, Wo, ws, rs);
    else if (b < 104) chain_red(lds, b - 64, ws, rs);
}

__global__ void __launch_bounds__(512) kP(char* ws) {
    const int s = blockIdx.x >> 2, hg = blockIdx.x & 3, tid = threadIdx.x, lane = tid & 63, w = tid >> 6, g = lane >> 4, l15 = lane & 15, h = hg * 8 + w;
    const u16* Q = (const u16*)(ws + OFF_Q); const u16* K = (const u16*)(ws + OFF_K); const u16* KM = (const u16*)(ws + OFF_KM);
    f32x4 sc[3][2];
#pragma unroll
    for (int a = 0; a < 3; ++a) for (int b = 0; b < 2; ++b) sc[a][b] = (f32x4){0.f, 0.f, 0.f, 0.f};
#pragma unroll
    for (int t = 0; t < 6; ++t) {
        bf16x8 qf[2], kf[3];
#pragma unroll
        for (int tt = 0; tt < 2; ++tt) qf[tt] = *(const bf16x8*)(Q + (size_t)(s * 32 + 16 * tt + l15) * HE + h * E_ + 32 * t + 8 * g);
        kf[0] = *(const bf16x8*)(KM + ((size_t)(s * 32 + h) * 8 + (lane & 7)) * E_ + 32 * t + 8 * g);
        kf[1] = *(const bf16x8*)(K + (size_t)(s * 32 + l15) * HE + h * E_ + 32 * t + 8 * g);
        kf[2] = *(const bf16x8*)(K + (size_t)(s * 32 + 16 + l15) * HE + h * E_ + 32 * t + 8 * g);
#pragma unroll
        for (int a = 0; a < 3; ++a)
#pragma unroll
            for (int tt = 0; tt < 2; ++tt) sc[a][tt] = mfma16(kf[a], qf[tt], sc[a][tt]);
    }
    const float scale = 0.07216878364870322f;
    u16* PT = (u16*)(ws + OFF_PT);
#pragma unroll
    for (int tt = 0; tt < 2; ++tt) {
        const int tok = 16 * tt + l15; float mx = -3.0e38f;
#pragma unroll
        for (int a = 0; a < 3; ++a)
#pragma unroll
            for (int r = 0; r < 4; ++r) {
                bool valid = (a == 0) ? (g < 2) : (16 * (a - 1) + 4 * g + r <= tok);
                float v = valid ? rbf(rbf(sc[a][tt][r]) * scale) : -3.0e38f; sc[a][tt][r] = v; mx = fmaxf(mx, v);
            }
        mx = fmaxf(mx, __shfl_xor(mx, 16)); mx = fmaxf(mx, __shfl_xor(mx, 32));
        float sum = 0.f;
#pragma unroll
        for (int a = 0; a < 3; ++a)
#pragma unroll
            for (int r = 0; r < 4; ++r) { float v = sc[a][tt][r]; v = (v > -1.0e38f) ? __expf(v - mx) : 0.f; sc[a][tt][r] = v; sum += v; }
        sum += __shfl_xor(sum, 16); sum += __shfl_xor(sum, 32);
        const float inv = 1.f / sum;
        u16* pr = PT + (size_t)(s * 32 + tok) * 1280 + h * 40;
        if (g < 2) *(v2u*)(pr + 4 * g) = (v2u){pk2(sc[0][tt][0] * inv, sc[0][tt][1] * inv), pk2(sc[0][tt][2] * inv, sc[0][tt][3] * inv)};
        *(v2u*)(pr + 8 + 4 * g) = (v2u){pk2(sc[1][tt][0] * inv, sc[1][tt][1] * inv), pk2(sc[1][tt][2] * inv, sc[1][tt][3] * inv)};
        *(v2u*)(pr + 24 + 4 * g) = (v2u){pk2(sc[2][tt][0] * inv, sc[2][tt][1] * inv), pk2(sc[2][tt][2] * inv, sc[2][tt][3] * inv)};
    }
}

__global__ void __launch_bounds__(512) kO(const u16* __restrict__ x, char* ws) {
    const int s = blockIdx.x / 5, cq = blockIdx.x % 5, tid = threadIdx.x, lane = tid & 63, w = tid >> 6, g = lane >> 4, l15 = lane & 15, r = cq * 8 + w;
    const u16* PT = (const u16*)(ws + OFF_PT);
    f32x4 acc[2] = {{0.f, 0.f, 0.f, 0.f}, {0.f, 0.f, 0.f, 0.f}};
#pragma unroll 4
    for (int T = 0; T < 40; ++T) {
        int cg = 4 * T + g, h = cg / 5, jg = cg % 5;
        const char* up = (jg == 0) ? (ws + OFF_UM + ((size_t)(s * 32 + h) * 40 + r) * 256 + l15 * 16)
                                   : (ws + OFF_UX + ((((size_t)(s * 32 + h) * 40 + r) * 4 + jg - 1) * 16 + l15) * 16);
        bf16x8 a = *(const bf16x8*)up;
#pragma unroll
        for (int tt = 0; tt < 2; ++tt) {
            bf16x8 b = *(const bf16x8*)(PT + (size_t)(s * 32 + 16 * tt + l15) * 1280 + cg * 8);
            acc[tt] = mfma16(a, b, acc[tt]);
        }
    }
    const float* mod = (const float*)(ws + OFF_MOD);
    u16* X1 = (u16*)(ws + OFF_X1);
#pragma unroll
    for (int tt = 0; tt < 2; ++tt) {
        int tok = s * 32 + 16 * tt + l15, c = 16 * r + 4 * g;
        v2u xv = *(const v2u*)(x + (size_t)tok * D_ + c);
        float xs[4] = {bflo(xv.x), bfhi(xv.x), bflo(xv.y), bfhi(xv.y)}; float o[4];
#pragma unroll
        for (int j = 0; j < 4; ++j) o[j] = xs[j] + rbf(mod[2 * 640 + c + j] * rbf(acc[tt][j]));
        *(v2u*)(X1 + (size_t)tok * D_ + c) = (v2u){pk2(o[0], o[1]), pk2(o[2], o[3])};
    }
}

__global__ void __launch_bounds__(512, 2) kF1(const u16* __restrict__ w1, const u16* __restrict__ w3, char* ws) {
    extern __shared__ __attribute__((aligned(16))) char lds[];
    const int rbk = blockIdx.x >> 4, ct = blockIdx.x & 15;
    const int tid = threadIdx.x, lane = tid & 63, w = tid >> 6, wm = w >> 1, wn = w & 1, g = lane >> 4, l15 = lane & 15;
    f32x4 acc[2][6];
    gemm128x192(lds, (const u16*)(ws + OFF_H2) + (size_t)rbk * 128 * D_, D_, D_,
                [&](int k, int ch) { int grp = ch / 6, cc = ch % 6; const u16* W = (grp & 1) ? w3 : w1; return W + (size_t)k * F_ + ct * 96 + (grp >> 1) * 48 + cc * 8; }, acc);
    u16* ACT = (u16*)(ws + OFF_ACT);
#pragma unroll
    for (int mt = 0; mt < 2; ++mt)
#pragma unroll
        for (int nt = 0; nt < 3; ++nt) {
            int tok = rbk * 128 + wm * 32 + mt * 16 + l15, f = ct * 96 + wn * 48 + nt * 16 + 4 * g; float o[4];
#pragma unroll
            for (int j = 0; j < 4; ++j) { float ga = rbf(acc[mt][nt][j]), up = rbf(acc[mt][nt + 3][j]); float sl = rbf(ga / (1.f + __expf(-ga))); o[j] = sl * up; }
            *(v2u*)(ACT + (size_t)tok * F_ + f) = (v2u){pk2(o[0], o[1]), pk2(o[2], o[3])};
        }
}
__global__ void __launch_bounds__(512, 2) kF2(const u16* __restrict__ w2, char* ws, u16* __restrict__ out) {
    extern __shared__ __attribute__((aligned(16))) char lds[];
    const int rbk = blockIdx.x >> 2, ct = blockIdx.x & 3;
    const int tid = threadIdx.x, lane = tid & 63, w = tid >> 6, wm = w >> 1, wn = w & 1, g = lane >> 4, l15 = lane & 15;
    f32x4 acc[2][6];
    gemm128x192(lds, (const u16*)(ws + OFF_ACT) + (size_t)rbk * 128 * F_, F_, F_,
                [&](int k, int ch) { int c = ct * 192 + ch * 8; if (c >= D_) c = 0; return w2 + (size_t)k * D_ + c; }, acc);
    const float* mod = (const float*)(ws + OFF_MOD); const u16* X1 = (const u16*)(ws + OFF_X1);
#pragma unroll
    for (int mt = 0; mt < 2; ++mt)
#pragma unroll
        for (int nt = 0; nt < 6; ++nt) {
            int tok = rbk * 128 + wm * 32 + mt * 16 + l15, c = ct * 192 + wn * 96 + nt * 16 + 4 * g;
            if (c < D_) {
                v2u xv = *(const v2u*)(X1 + (size_t)tok * D_ + c);
                float xs[4] = {bflo(xv.x), bfhi(xv.x), bflo(xv.y), bfhi(xv.y)}; float o[4];
#pragma unroll
                for (int j = 0; j < 4; ++j) o[j] = xs[j] + rbf(mod[5 * 640 + c + j] * rbf(acc[mt][nt][j]));
                *(v2u*)(out + (size_t)tok * D_ + c) = (v2u){pk2(o[0], o[1]), pk2(o[2], o[3])};
            }
        }
}

extern "C" void kernel_launch(void* const* d_in, const int* in_sizes, int n_in, void* d_out, int out_size, void* d_ws, size_t ws_size, hipStream_t stream) {
    const u16* x = (const u16*)d_in[0]; const u16* mem0 = (const u16*)d_in[1];
    const u16* Wq = (const u16*)d_in[2]; const u16* Wk = (const u16*)d_in[3]; const u16* Wv = (const u16*)d_in[4]; const u16* Wo = (const u16*)d_in[5];
    const u16* w1 = (const u16*)d_in[8]; const u16* w3 = (const u16*)d_in[9]; const u16* w2 = (const u16*)d_in[10];
    const u16* c = (const u16*)d_in[11]; const u16* wmod = (const u16*)d_in[12];
    char* ws = (char*)d_ws; u16* out = (u16*)d_out;
    hipFuncSetAttribute((const void*)kA, hipFuncAttributeMaxDynamicSharedMemorySize, KA_LDS);
    hipFuncSetAttribute((const void*)k_chain, hipFuncAttributeMaxDynamicSharedMemorySize, C_LDS);
    hipFuncSetAttribute((const void*)kF1, hipFuncAttributeMaxDynamicSharedMemorySize, 2 * GBUF);
    hipFuncSetAttribute((const void*)kF2, hipFuncAttributeMaxDynamicSharedMemorySize, 2 * GBUF);
    k_pre<<<256, 256, 0, stream>>>(c, wmod, mem0, ws);
    k_ln<<<S_ / 4, 256, 0, stream>>>(x, (const float*)(ws + OFF_MOD), 0, 640, (u16*)(ws + OFF_H));
    kA<<<512, 512, KA_LDS, stream>>>(Wq, Wk, Wv, Wo, ws);
    k_chain<<<104, 512, C_LDS, stream>>>(Wq, Wk, Wv, Wo, ws);
    kP<<<256, 512, 0, stream>>>(ws);
    kO<<<320, 512, 0, stream>>>(x, ws);
    k_ln<<<S_ / 4, 256, 0, stream>>>((const u16*)(ws + OFF_X1), (const float*)(ws + OFF_MOD), 3 * 640, 4 * 640, (u16*)(ws + OFF_H2));
    kF1<<<256, 512, 2 * GBUF, stream>>>(w1, w3, ws);
    kF2<<<64, 512, 2 * GBUF, stream>>>(w2, ws, out);
}
```

```cpp
#include <hip/hip_runtime.h>
#include <hip/hip_bf16.h>
#include <stdint.h>

#define LAS __attribute__((address_space(3)))
#define GAS __attribute__((address_space(1)))
typedef short bf16x8 __attribute__((ext_vector_type(8)));
typedef short bf16x4 __attribute__((ext_vector_type(4)));
typedef float f32x4 __attribute__((ext_vector_type(4)));
typedef unsigned v4u __attribute__((ext_vector_type(4)));
typedef unsigned v2u __attribute__((ext_vector_type(2)));
typedef unsigned short u16;
typedef GAS unsigned gu32;
#define RLX_AGENT __ATOMIC_RELAXED, __HIP_MEMORY_SCOPE_AGENT

constexpr int S_ = 2048, D_ = 640, H_ = 32, E_ = 192, HE = 6144, NSEG = 64, F_ = 1536;
constexpr size_t OFF_MOD = 0;
constexpr size_t OFF_CTL = 16384;
constexpr size_t OFF_TAB = 49152;
constexpr size_t OFF_H   = 65536;
constexpr size_t OFF_Q   = OFF_H + (size_t)S_ * D_ * 2;
constexpr size_t OFF_K   = OFF_Q + (size_t)S_ * HE * 2;
constexpr size_t OFF_UX  = OFF_K + (size_t)S_ * HE * 2;
constexpr size_t OFF_MEM = OFF_UX + (size_t)NSEG * H_ * 40 * 4 * 256;
constexpr size_t OFF_P   = OFF_MEM + (size_t)65 * 10240;
constexpr size_t OFF_UM  = OFF_P + (size_t)NSEG * 20480;
constexpr size_t OFF_KM  = OFF_UM + (size_t)NSEG * H_ * 40 * 256;
constexpr size_t OFF_PT  = OFF_KM + (size_t)NSEG * H_ * 8 * E_ * 2;
constexpr size_t OFF_X1  = OFF_PT + (size_t)S_ * 1280 * 2;
constexpr size_t OFF_ACT = OFF_X1 + (size_t)S_ * D_ * 2;
constexpr size_t OFF_END = OFF_ACT + (size_t)S_ * F_ * 2;
constexpr unsigned SENT = 0xFFFFFFFFu;
constexpr unsigned kSpinCap = 1u << 20;
constexpr int CW_QUEUE = 0, CW_TMO = 32, CW_A = 64  , CW_Q = CW_A + 32 * 16  , CW_P = CW_Q + 32 * 16  , CW_X = CW_P + 32 * 64, CW_F = CW_X + 32 * 64, CW_END = CW_F + 32 * 64;
static_assert(CW_END * 4 <= 32768, "ctl");
#define ST(role_idx, s_, k_) do {} while (0)

__device__ __forceinline__ float bf2f(u16 v) { return __uint_as_float((unsigned)v << 16); }
__device__ __forceinline__ float bflo(unsigned v) { return __uint_as_float(v << 16); }
__device__ __forceinline__ float bfhi(unsigned v) { return __uint_as_float(v & 0xFFFF0000u); }
typedef __bf16 nbf2 __attribute__((ext_vector_type(2)));
__device__ __forceinline__ unsigned pk2(float a, float b) { nbf2 t = {(__bf16)a, (__bf16)b}; return __builtin_bit_cast(unsigned, t); }
__device__ __forceinline__ float rbf(float a) { return bflo(pk2(a, 0.f)); }
__device__ __forceinline__ f32x4 mfma16(bf16x8 a, bf16x8 b, f32x4 c) { return __builtin_amdgcn_mfma_f32_16x16x32_bf16(a, b, c, 0, 0, 0); }
__device__ __forceinline__ bf16x4 trread(const char* lds_base, int off) { return __builtin_amdgcn_ds_read_tr16_b64_v4i16((LAS bf16x4*)(lds_base + off)); }
__device__ __forceinline__ bf16x8 cat8(bf16x4 a, bf16x4 b) { return __builtin_shufflevector(a, b, 0, 1, 2, 3, 4, 5, 6, 7); }
__device__ __forceinline__ v4u ld16c(__amdgpu_buffer_rsrc_t r, unsigned off) { return __builtin_amdgcn_raw_buffer_load_b128(r, off, 0, 16); }
__device__ __forceinline__ v2u ld8c(__amdgpu_buffer_rsrc_t r, unsigned off) { return __builtin_amdgcn_raw_buffer_load_b64(r, off, 0, 16); }
__device__ __forceinline__ void st16c(__amdgpu_buffer_rsrc_t r, unsigned off, v4u v) { __builtin_amdgcn_raw_buffer_store_b128(v, r, off, 0, 16); }
__device__ __forceinline__ bool okv(v4u v) { return v.x != SENT && v.y != SENT && v.z != SENT && v.w != SENT; }
__device__ __forceinline__ bf16x8 asf(v4u v) { return __builtin_bit_cast(bf16x8, v); }
#define SB() __builtin_amdgcn_sched_barrier(0)
__device__ __forceinline__ float xmax16(float x) { unsigned u = __float_as_uint(x); auto r = __builtin_amdgcn_permlane16_swap(u, u, false, false); return fmaxf(__uint_as_float(r[0]), __uint_as_float(r[1])); }
__device__ __forceinline__ float xmax32(float x) { unsigned u = __float_as_uint(x); auto r = __builtin_amdgcn_permlane32_swap(u, u, false, false); return fmaxf(__uint_as_float(r[0]), __uint_as_float(r[1])); }
__device__ __forceinline__ float xsum16(float x) { unsigned u = __float_as_uint(x); auto r = __builtin_amdgcn_permlane16_swap(u, u, false, false); return __uint_as_float(r[0]) + __uint_as_float(r[1]); }
__device__ __forceinline__ float xsum32(float x) { unsigned u = __float_as_uint(x); auto r = __builtin_amdgcn_permlane32_swap(u, u, false, false); return __uint_as_float(r[0]) + __uint_as_float(r[1]); }
__device__ __forceinline__ v4u pair16(f32x4 a, f32x4 b) {
    unsigned a0 = pk2(a[0], a[1]), a1 = pk2(a[2], a[3]), b0 = pk2(b[0], b[1]), b1 = pk2(b[2], b[3]);
    auto x0 = __builtin_amdgcn_permlane16_swap(a0, b0, false, false); auto x1 = __builtin_amdgcn_permlane16_swap(a1, b1, false, false);
    return (v4u){x0[0], x1[0], x0[1], x1[1]};
}
__device__ __forceinline__ void bar_lds() { asm volatile("s_waitcnt lgkmcnt(0)\n\ts_barrier" ::: "memory"); }
__device__ __forceinline__ void drain_vm() { asm volatile("s_waitcnt vmcnt(0)" ::: "memory"); }
__device__ __forceinline__ bool wait_cnt(gu32* ctl, int idx, unsigned target) {
    for (unsigned spins = 0;; ++spins) {
        asm volatile("" ::: "memory");
        unsigned v = __builtin_amdgcn_readfirstlane(__hip_atomic_load(ctl + idx, RLX_AGENT));
        if (v >= target) return false;
        if (spins > kSpinCap) { __hip_atomic_store(ctl + CW_TMO, 1u, RLX_AGENT); return true; }
        if ((spins & 63u) == 63u && __builtin_amdgcn_readfirstlane(__hip_atomic_load(ctl + CW_TMO, RLX_AGENT))) return true;
        __builtin_amdgcn_s_sleep(2);
    }
}

__device__ __forceinline__ int boff192(int row, int ch) { int f = ((row >> 1) & 1) | (((row >> 3) & 1) << 1); return row * 384 + (((ch >> 1) ^ f) << 5) + ((ch & 1) << 4); }
template <int RB> __device__ __forceinline__ int boffP(int row, int ch) { int f = (row & 3) | (((row >> 3) & 1) << 2); return row * RB + (((ch >> 1) ^ f) << 5) + ((ch & 1) << 4); }
__device__ __forceinline__ bf16x8 trfrag192(const char* base, int r0, int c, int lane) {
    const int g = lane >> 4, q = (lane & 15) >> 2, p = lane & 3;
    const int f = ((q >> 1) & 1) | ((g & 1) << 1);
    const int a = (r0 + 8 * g + q) * 384 + ((c ^ f) << 5) + 8 * p;
    return cat8(trread(base, a), trread(base, a + 4 * 384));
}
template <int RB> __device__ __forceinline__ bf16x8 trfragP(const char* base, int r0, int c, int lane) {
    const int g = lane >> 4, q = (lane & 15) >> 2, p = lane & 3;
    const int f = q | ((g & 1) << 2);
    const int a = (r0 + 8 * g + q) * RB + ((c ^ f) << 5) + 8 * p;
    return cat8(trread(base, a), trread(base, a + 4 * RB));
}

__host__ __device__ constexpr int slot_size(int t) {
    int n = 0;
    if (t < 64) n += 4;
    if (t >= 1 && t <= 64) n += 5;
    { int s = t - 2; if (s >= 0 && s < 60) n += (s % 4 == 3) ? 16 : 0; else if (s >= 60 && s < 64) n += 32; }
    { int s = t - 3; if (s >= 0 && s < 60) n += (s % 4 == 3) ? 4 : 0; else if (s >= 60 && s < 64) n += 10; }
    return n;
}
__host__ __device__ constexpr int slot_task(int t, int v) {
    if (t < 64) { if (v < 4) return (2 << 16) | (t << 8) | v; v -= 4; }
    if (t >= 1 && t <= 64) { if (v < 5) return (3 << 16) | ((t - 1) << 8) | v; v -= 5; }
    { int s = t - 2; if (s >= 0 && s < 60) { if (s % 4 == 3) { if (v < 16) return (6 << 16) | ((s >> 2) << 8) | v; v -= 16; } } else if (s >= 60 && s < 64) { if (v < 32) return (4 << 16) | (s << 8) | v; v -= 32; } }
    { int s = t - 3; if (s >= 0 && s < 60) { if (s % 4 == 3) return (7 << 16) | ((s >> 2) << 8) | v; } else return (5 << 16) | (s << 8) | v; }
    return -1;
}
__host__ __device__ constexpr int group_size(int r) {
    int n = 0; if (r < 16) n += 32; if (r - 3 >= 0 && r - 3 < 16) n += 32;
    for (int j = 0; j < 4; ++j) { int t = 4 * (r - 4) + j; if (t >= 0 && t < 67) n += slot_size(t); }
    return n;
}
__host__ __device__ constexpr int total_tasks() { int n = 0; for (int r = 0; r < 22; ++r) n += group_size(r); return n; }
constexpr int NTASK = total_tasks();
static_assert(NTASK <= 4096, "task table");
__device__ void build_group(int r, int* tab) {
    int pos = 0; for (int q = 0; q < r; ++q) pos += group_size(q);
    if (r < 16) { for (int h = 0; h < 32; ++h) tab[pos++] = (0 << 16) | (r << 8) | h; }
    if (r - 3 >= 0 && r - 3 < 16) { for (int h = 0; h < 32; ++h) tab[pos++] = (1 << 16) | ((r - 3) << 8) | h; }
    for (int j = 0; j < 4; ++j) { int t = 4 * (r - 4) + j; if (t >= 0 && t < 67) { int n = slot_size(t); for (int v = 0; v < n; ++v) tab[pos++] = slot_task(t, v); } }
}

__global__ void __launch_bounds__(256) k_pre(const u16* __restrict__ c, const u16* __restrict__ wmod, const u16* __restrict__ mem0, char* ws) {
    __shared__ float sc[640]; __shared__ float red[64][33];
    const int b = blockIdx.x, tid = threadIdx.x;
    if (b < 120) {
        for (int i = tid; i < 640; i += 256) { float v = bf2f(c[i]); sc[i] = rbf(v / (1.f + __expf(-v))); }
        __syncthreads();
        const int kg = tid >> 2, cq = tid & 3;
        v4u wv[10];
#pragma unroll
        for (int j = 0; j < 10; ++j) wv[j] = *(const v4u*)(wmod + (size_t)(kg * 10 + j) * 3840 + b * 32 + cq * 8);
        float a[8] = {0.f, 0.f, 0.f, 0.f, 0.f, 0.f, 0.f, 0.f};
#pragma unroll
        for (int j = 0; j < 10; ++j) { float s = sc[kg * 10 + j];
            a[0] += s * bflo(wv[j].x); a[1] += s * bfhi(wv[j].x); a[2] += s * bflo(wv[j].y); a[3] += s * bfhi(wv[j].y);
            a[4] += s * bflo(wv[j].z); a[5] += s * bfhi(wv[j].z); a[6] += s * bflo(wv[j].w); a[7] += s * bfhi(wv[j].w); }
#pragma unroll
        for (int j = 0; j < 8; ++j) red[kg][cq * 8 + j] = a[j];
        __syncthreads();
        if (tid < 32) { float s = 0.f; for (int k = 0; k < 64; ++k) s += red[k][tid]; ((float*)(ws + OFF_MOD))[b * 32 + tid] = rbf(s); }
    } else if (b == 121) {
        if (tid < 22) build_group(tid, (int*)(ws + OFF_TAB));
    } else if (b == 120) {
        for (int ci = tid; ci < 640; ci += 256) { int i = ci & 7, kg = ci >> 3; *(v4u*)(ws + OFF_MEM + ci * 16) = *(const v4u*)(mem0 + i * 640 + kg * 8); }
        for (int i = tid; i < 8192; i += 256) ((unsigned*)(ws + OFF_CTL))[i] = 0;
    }
    const size_t beg = OFF_MEM + 10240, n16 = (OFF_PT - beg) / 16;
    v4u sv = {SENT, SENT, SENT, SENT};
    for (size_t i = (size_t)b * 256 + tid; i < n16; i += (size_t)gridDim.x * 256) *(v4u*)(ws + beg + i * 16) = sv;
}

__global__ void __launch_bounds__(256) k_ln(const u16* __restrict__ x, const float* __restrict__ mod, int so, int co, u16* __restrict__ out) {
    const int row = blockIdx.x * 4 + (threadIdx.x >> 6), lane = threadIdx.x & 63;
    const unsigned* xr = (const unsigned*)(x + (size_t)row * D_);
    float v[10]; float s = 0.f;
#pragma unroll
    for (int j = 0; j < 5; ++j) { unsigned u = xr[j * 64 + lane]; v[2 * j] = bflo(u); v[2 * j + 1] = bfhi(u); s += v[2 * j] + v[2 * j + 1]; }
#pragma unroll
    for (int m = 32; m >= 1; m >>= 1) s += __shfl_xor(s, m);
    const float mean = s * (1.f / D_); float q = 0.f;
#pragma unroll
    for (int j = 0; j < 10; ++j) { float d = v[j] - mean; q += d * d; }
#pragma unroll
    for (int m = 32; m >= 1; m >>= 1) q += __shfl_xor(q, m);
    const float rstd = 1.f / sqrtf(q * (1.f / D_) + 1e-5f);
    unsigned* orow = (unsigned*)(out + (size_t)row * D_);
#pragma unroll
    for (int j = 0; j < 5; ++j) { int c0 = (j * 64 + lane) * 2;
        float a = (v[2 * j] - mean) * rstd * (1.f + mod[co + c0]) + mod[so + c0];
        float b = (v[2 * j + 1] - mean) * rstd * (1.f + mod[co + c0 + 1]) + mod[so + c0 + 1];
        orow[j * 64 + lane] = pk2(a, b); }
}

constexpr int GA_B = 128 * 128;
constexpr int GB_B = 64 * 384;
constexpr int GBUF = GA_B + GB_B;
#define PFD 2
#define PFD2 2
template <int NK, int PD, int AUXA, class AT>
__device__ __forceinline__ void gemm4(char* lds, __amdgpu_buffer_rsrc_t rsA, unsigned vA, unsigned sA, unsigned lda2,
                                      __amdgpu_buffer_rsrc_t rsB0, __amdgpu_buffer_rsrc_t rsB1, unsigned vB0, unsigned vB1, unsigned sB, unsigned ldb2, int cB0, int cB1,
                                      AT atr, f32x4 (&acc)[4][6]) {
    int tid = threadIdx.x; asm volatile("" : "+v"(tid));
    const int lane = tid & 63, w = tid >> 6, wm = w >> 1, wn = w & 1, g = lane >> 4, l15 = lane & 15;
#pragma unroll
    for (int a = 0; a < 4; ++a)
#pragma unroll
        for (int b = 0; b < 6; ++b) acc[a][b] = (f32x4){0.f, 0.f, 0.f, 0.f};
    v4u ra[PD][4], rb[PD][8];
    const int ar = tid >> 3, ach = tid & 7, br = (tid < 192 ? tid : 0) / 12;
    const int bf = ((br >> 1) & 1) | (((br >> 3) & 1) << 1);
    const int bo0 = GA_B + br * 384 + (((cB0 >> 1) ^ bf) << 5) + ((cB0 & 1) << 4);
    const int bo1 = GA_B + br * 384 + (((cB1 >> 1) ^ bf) << 5) + ((cB1 & 1) << 4);
    const int ao = ar * 128 + ((ach ^ (ar & 7)) << 4);
    auto gload = [&](int kc, v4u (&a4)[4], v4u (&b8)[8]) {
#pragma unroll
        for (int i = 0; i < 4; ++i) a4[i] = __builtin_amdgcn_raw_buffer_load_b128(rsA, vA, sA + kc * 128 + i * 32 * lda2, AUXA);
#pragma unroll
        for (int i = 0; i < 4; ++i) {
            b8[i] = __builtin_amdgcn_raw_buffer_load_b128(rsB0, vB0, sB + (kc * 64 + 16 * i) * ldb2, 0);
            b8[4 + i] = __builtin_amdgcn_raw_buffer_load_b128(rsB1, vB1, sB + (kc * 64 + 16 * i) * ldb2, 0);
        }
    };
    auto lstore = [&](int kc, char* buf, v4u (&a4)[4], v4u (&b8)[8]) {
#pragma unroll
        for (int i = 0; i < 4; ++i) *(v4u*)(buf + ao + i * 4096) = atr(kc, ar + 32 * i, ach, a4[i]);
        if (tid < 192) {
#pragma unroll
            for (int i = 0; i < 4; ++i) { *(v4u*)(buf + bo0 + i * 6144) = b8[i]; *(v4u*)(buf + bo1 + i * 6144) = b8[4 + i]; }
        }
    };
#pragma unroll
    for (int k = 0; k < PD; ++k) gload(k, ra[k], rb[k]);
    lstore(0, lds, ra[0], rb[0]);
    if (PD < NK) gload(PD, ra[0], rb[0]);
    __syncthreads();
#pragma unroll 1
    for (int k0 = 0; k0 < NK; k0 += PD) {
#pragma unroll
        for (int u = 0; u < PD; ++u) {
            const int kc = k0 + u;
            char* cur = lds + (kc & 1) * GBUF; char* nxt = lds + ((kc + 1) & 1) * GBUF;
#pragma unroll
            for (int ks = 0; ks < 2; ++ks) {
                bf16x8 af[4], bfr[6];
#pragma unroll
                for (int mt = 0; mt < 4; ++mt) { int r = wm * 64 + mt * 16 + l15; af[mt] = *(const bf16x8*)(cur + r * 128 + (((4 * ks + g) ^ (r & 7)) << 4)); }
#pragma unroll
                for (int nt = 0; nt < 6; ++nt) bfr[nt] = trfrag192(cur + GA_B, 32 * ks, wn * 6 + nt, lane);
#pragma unroll
                for (int mt = 0; mt < 4; ++mt)
#pragma unroll
                    for (int nt = 0; nt < 6; ++nt) acc[mt][nt] = mfma16(bfr[nt], af[mt], acc[mt][nt]);
            }
            if (kc + 1 < NK) {
                lstore(kc + 1, nxt, ra[(u + 1) % PD], rb[(u + 1) % PD]);
                if (kc + 1 + PD < NK) gload(kc + 1 + PD, ra[(u + 1) % PD], rb[(u + 1) % PD]);
            }
            __syncthreads();
        }
    }
}
__device__ __forceinline__ __amdgpu_buffer_rsrc_t mkrs(const void* p, unsigned bytes) { return __builtin_amdgcn_make_buffer_rsrc((void*)p, 0, (int)bytes, 0x00020000); }

constexpr int KA_VOFF = 2 * GBUF;
constexpr int KA_VSTR = 400;
__device__ __forceinline__ void taskA(char* lds, int rbk, int h, int qonly, const u16* __restrict__ Wq, const u16* __restrict__ Wk, const u16* __restrict__ Wv,
                                      const u16* __restrict__ Wo, char* ws, __amdgpu_buffer_rsrc_t rw) {
    int tid = threadIdx.x; asm volatile("" : "+v"(tid));
    const int lane = tid & 63, w = tid >> 6, wm = w >> 1, wn = w & 1, g = lane >> 4, l15 = lane & 15;
    f32x4 acc[4][6];
#pragma unroll 1
    for (int mat = qonly ? 1 : 0; mat < 3; mat += 2) {
        const __amdgpu_buffer_rsrc_t rW = mkrs(mat == 0 ? Wk : (mat == 1 ? Wq : Wv), (unsigned)(D_ * HE * 2));
        const int bt = tid < 192 ? tid : 0, brr = bt / 12, bc = bt % 12;
        gemm4<10, PFD, 0>(lds, rw, (unsigned)(((tid >> 3) * D_ + (tid & 7) * 8) * 2), (unsigned)OFF_H + (unsigned)(rbk * 128 * D_ * 2), D_ * 2,
                          rW, rW, (unsigned)((brr * HE + bc * 8) * 2), (unsigned)((brr * HE + (12 + bc) * 8) * 2), (unsigned)(h * E_ * 2), HE * 2, bc, 12 + bc,
                          [](int, int, int, v4u v) { return v; }, acc);
        if (mat < 2) {
            const unsigned ob = (unsigned)(mat == 0 ? OFF_K : OFF_Q);
#pragma unroll
            for (int mt = 0; mt < 4; ++mt)
#pragma unroll
                for (int nt = 0; nt < 6; nt += 2) {
                    int tok = rbk * 128 + wm * 64 + mt * 16 + l15, e = wn * 96 + (nt + (g & 1)) * 16 + 8 * (g >> 1);
                    st16c(rw, ob + (unsigned)(tok * HE + h * E_ + e) * 2u, pair16(acc[mt][nt], acc[mt][nt + 1]));
                }
        } else {
#pragma unroll
            for (int mt = 0; mt < 4; ++mt)
#pragma unroll
                for (int nt = 0; nt < 6; ++nt) {
                    int tok = wm * 64 + mt * 16 + l15, e = wn * 96 + nt * 16 + 4 * g;
                    *(v2u*)(lds + KA_VOFF + tok * KA_VSTR + e * 2) = (v2u){pk2(acc[mt][nt][0], acc[mt][nt][1]), pk2(acc[mt][nt][2], acc[mt][nt][3])};
                }
        }
    }
    if (qonly) return;
    __syncthreads();
    bf16x8 vf[4][6];
#pragma unroll
    for (int mt = 0; mt < 4; ++mt)
#pragma unroll
        for (int t = 0; t < 6; ++t) vf[mt][t] = *(const bf16x8*)(lds + KA_VOFF + (wm * 64 + mt * 16 + l15) * KA_VSTR + (4 * t + g) * 16);
    const __amdgpu_buffer_rsrc_t rWo = mkrs(Wo, (unsigned)(HE * D_ * 2));
    const int r0 = tid >> 4, c0 = tid & 15;
    const int wof = r0 * 256 + (((c0 >> 1) ^ ((r0 & 3) | (((r0 >> 3) & 1) << 2))) << 5) + ((c0 & 1) << 4);
    v4u rb[12];
    auto gl = [&](int nc) {
#pragma unroll
        for (int i = 0; i < 12; ++i) rb[i] = __builtin_amdgcn_raw_buffer_load_b128(rWo, (unsigned)((r0 * D_ + c0 * 8) * 2), (unsigned)(((h * E_ + 16 * i) * D_ + nc * 128) * 2), 0);
    };
    gl(0);
#pragma unroll 1
    for (int nc = 0; nc < 5; ++nc) {
        __syncthreads();
#pragma unroll
        for (int i = 0; i < 12; ++i) *(v4u*)(lds + wof + i * 4096) = rb[i];
        __syncthreads();
        if (nc + 1 < 5) gl(nc + 1);
        f32x4 ua[4][4];
#pragma unroll
        for (int mt = 0; mt < 4; ++mt)
#pragma unroll
            for (int nt = 0; nt < 4; ++nt) ua[mt][nt] = (f32x4){0.f, 0.f, 0.f, 0.f};
#pragma unroll
        for (int t = 0; t < 6; ++t) {
            bf16x8 wf[4];
#pragma unroll
            for (int nt = 0; nt < 4; ++nt) wf[nt] = trfragP<256>(lds, 32 * t, wn * 4 + nt, lane);
#pragma unroll
            for (int mt = 0; mt < 4; ++mt)
#pragma unroll
                for (int nt = 0; nt < 4; ++nt) ua[mt][nt] = mfma16(vf[mt][t], wf[nt], ua[mt][nt]);
        }
#pragma unroll
        for (int mt = 0; mt < 4; ++mt)
#pragma unroll
            for (int nt = 0; nt < 4; nt += 2) {
                int seg = rbk * 4 + wm * 2 + (mt >> 1), r = nc * 8 + wn * 4 + nt + (g & 1), jg = 2 * (mt & 1) + (g >> 1);
                unsigned off = (unsigned)OFF_UX + (unsigned)(((((seg * 32 + h) * 40 + r) * 4 + jg) * 16 + l15) * 16);
                st16c(rw, off, pair16(ua[mt][nt], ua[mt][nt + 1]));
            }
    }
}

__device__ __forceinline__ bool taskP(int s, int hq, char* ws, __amdgpu_buffer_rsrc_t rw) {
    int tid = threadIdx.x; asm volatile("" : "+v"(tid));
    const int lane = tid & 63, w = tid >> 6, g = lane >> 4, l15 = lane & 15;
    const float scale = 0.07216878364870322f;
    bool dead = false;
    v4u qf[2][6][2], kf[2][6][2], km[2][6];
#pragma unroll
    for (int hh = 0; hh < 2; ++hh) {
        const int h = hq * 8 + w * 2 + hh;
#pragma unroll
        for (int t = 0; t < 6; ++t)
#pragma unroll
            for (int tt = 0; tt < 2; ++tt) {
                unsigned o = (unsigned)(((s * 32 + 16 * tt + l15) * HE + h * E_ + 32 * t + 8 * g) * 2);
                qf[hh][t][tt] = ld16c(rw, (unsigned)OFF_Q + o); kf[hh][t][tt] = ld16c(rw, (unsigned)OFF_K + o);
            }
    }
    {
        unsigned spins = 0;
        const unsigned kb = (unsigned)OFF_KM + (unsigned)((((s * 32 + hq * 8 + w * 2) * 8 + (lane & 7)) * E_ + 8 * g) * 2);
        while (true) {
            asm volatile("" ::: "memory");
            bool ok = true;
#pragma unroll
            for (int hh = 0; hh < 2; ++hh)
#pragma unroll
                for (int t = 0; t < 6; ++t) km[hh][t] = ld16c(rw, kb + hh * (8 * E_ * 2) + t * 64);
#pragma unroll
            for (int hh = 0; hh < 2; ++hh)
#pragma unroll
                for (int t = 0; t < 6; ++t) ok &= okv(km[hh][t]);
            if (__all(ok)) break;
            if (++spins > kSpinCap) { dead = true; break; }
            __builtin_amdgcn_s_sleep(4);
        }
    }
#pragma unroll
    for (int hh = 0; hh < 2; ++hh) {
        const int h = hq * 8 + w * 2 + hh;
        f32x4 sc[3][2];
#pragma unroll
        for (int a = 0; a < 3; ++a) for (int b = 0; b < 2; ++b) sc[a][b] = (f32x4){0.f, 0.f, 0.f, 0.f};
#pragma unroll
        for (int t = 0; t < 6; ++t)
#pragma unroll
            for (int tt = 0; tt < 2; ++tt) {
                sc[0][tt] = mfma16(asf(km[hh][t]), asf(qf[hh][t][tt]), sc[0][tt]);
                sc[1][tt] = mfma16(asf(kf[hh][t][0]), asf(qf[hh][t][tt]), sc[1][tt]);
                sc[2][tt] = mfma16(asf(kf[hh][t][1]), asf(qf[hh][t][tt]), sc[2][tt]);
            }
#pragma unroll
        for (int tt = 0; tt < 2; ++tt) {
            const int tok = 16 * tt + l15; float mx = -3.0e38f;
#pragma unroll
            for (int a = 0; a < 3; ++a)
#pragma unroll
                for (int r = 0; r < 4; ++r) {
                    bool valid = (a == 0) ? (g < 2) : (16 * (a - 1) + 4 * g + r <= tok);
                    float v = valid ? rbf(rbf(sc[a][tt][r]) * scale) : -3.0e38f; sc[a][tt][r] = v; mx = fmaxf(mx, v);
                }
            mx = xmax32(xmax16(mx));
            float sum = 0.f;
#pragma unroll
            for (int a = 0; a < 3; ++a)
#pragma unroll
                for (int r = 0; r < 4; ++r) { float v = __expf(sc[a][tt][r] - mx); sc[a][tt][r] = v; sum += v; }
            sum = xsum32(xsum16(sum));
            const float inv = 1.f / sum;
            const unsigned pr = (unsigned)OFF_PT + (unsigned)(((s * 32 + tok) * 1280 + h * 40) * 2);
            v4u x = pair16(sc[1][tt] * inv, sc[2][tt] * inv), y = pair16(sc[0][tt] * inv, sc[0][tt] * inv);
            st16c(rw, pr + (1 + (g >> 1) + 2 * (g & 1)) * 16, x);
            if (g == 0) st16c(rw, pr, y);
        }
    }
    return dead;
}

__device__ __forceinline__ bool taskO(int s, int cq, const u16* __restrict__ x, char* ws, __amdgpu_buffer_rsrc_t rw) {
    int tid = threadIdx.x; asm volatile("" : "+v"(tid));
    const int lane = tid & 63, w = tid >> 6, g = lane >> 4, l15 = lane & 15;
    const int r0 = cq * 8 + w * 2;
    f32x4 acc[2][2];
#pragma unroll
    for (int a = 0; a < 2; ++a) for (int b = 0; b < 2; ++b) acc[a][b] = (f32x4){0.f, 0.f, 0.f, 0.f};
    bool dead = false;
    v4u ua[2][5][2], pb[2][5][2];
    auto ldb = [&](int T0, v4u (&u)[5][2], v4u (&p)[5][2]) {
#pragma unroll
        for (int t = 0; t < 5; ++t) {
            int cg = 4 * (T0 + t) + g, h = cg / 5, jg = cg % 5;
#pragma unroll
            for (int n = 0; n < 2; ++n) {
                unsigned uo = (jg == 0) ? (unsigned)OFF_UM + (unsigned)(((s * 32 + h) * 40 + r0 + n) * 256 + l15 * 16)
                                        : (unsigned)OFF_UX + (unsigned)(((((s * 32 + h) * 40 + r0 + n) * 4 + jg - 1) * 16 + l15) * 16);
                u[t][n] = ld16c(rw, uo);
            }
#pragma unroll
            for (int tt = 0; tt < 2; ++tt) p[t][tt] = ld16c(rw, (unsigned)OFF_PT + (unsigned)(((s * 32 + 16 * tt + l15) * 1280 + cg * 8) * 2));
        }
    };
    ldb(0, ua[0], pb[0]);
#pragma unroll
    for (int b = 0; b < 8; ++b) {
        if (b + 1 < 8) ldb(5 * (b + 1), ua[(b + 1) & 1], pb[(b + 1) & 1]);
        unsigned spins = 0;
        while (true) {
            bool ok = true;
#pragma unroll
            for (int t = 0; t < 5; ++t) { bool um = ((4 * (5 * b + t) + g) % 5 == 0); ok &= (okv(ua[b & 1][t][0]) && okv(ua[b & 1][t][1])) || !um; }
            if (__all(ok)) break;
            if (++spins > kSpinCap) { dead = true; break; }
            __builtin_amdgcn_s_sleep(4);
            asm volatile("" ::: "memory");
            ldb(5 * b, ua[b & 1], pb[b & 1]);
        }
#pragma unroll
        for (int t = 0; t < 5; ++t)
#pragma unroll
            for (int n = 0; n < 2; ++n)
#pragma unroll
                for (int tt = 0; tt < 2; ++tt) acc[n][tt] = mfma16(asf(ua[b & 1][t][n]), asf(pb[b & 1][t][tt]), acc[n][tt]);
    }
    const float* mod = (const float*)(ws + OFF_MOD);
#pragma unroll
    for (int tt = 0; tt < 2; ++tt) {
        f32x4 o[2];
#pragma unroll
        for (int n = 0; n < 2; ++n) {
            int tok = s * 32 + 16 * tt + l15, c = 16 * (r0 + n) + 4 * g;
            v2u xv = *(const v2u*)(x + (size_t)tok * D_ + c);
            float xs[4] = {bflo(xv.x), bfhi(xv.x), bflo(xv.y), bfhi(xv.y)};
#pragma unroll
            for (int j = 0; j < 4; ++j) o[n][j] = xs[j] + rbf(mod[2 * 640 + c + j] * rbf(acc[n][tt][j]));
        }
        int tok = s * 32 + 16 * tt + l15, c = 16 * (r0 + (g & 1)) + 8 * (g >> 1);
        st16c(rw, (unsigned)OFF_X1 + (unsigned)((tok * D_ + c) * 2), pair16(o[0], o[1]));
    }
    return dead;
}

constexpr int F1_APITCH = 1296;
constexpr int F1_BOFF = 32 * F1_APITCH;
constexpr int F1_ROFF = F1_BOFF;
__device__ __forceinline__ void taskF1(char* lds, int s, int ct, const u16* __restrict__ w1, const u16* __restrict__ w3, char* ws, __amdgpu_buffer_rsrc_t rw) {
    int tid = threadIdx.x; asm volatile("" : "+v"(tid));
    const int lane = tid & 63, w = tid >> 6, g = lane >> 4, l15 = lane & 15;
    const __amdgpu_buffer_rsrc_t r1 = mkrs(w1, D_ * F_ * 2), r3 = mkrs(w3, D_ * F_ * 2);
    v4u bw[5][6];
    unsigned vo[3]; int lo[3];
#pragma unroll
    for (int i = 0; i < 3; ++i) { int idx = lane + 64 * i, row = idx / 6, c = idx % 6; vo[i] = (unsigned)((row * F_ + c * 8) * 2);
        lo[i] = row * 192 + c * 16; }
#pragma unroll
    for (int j = 0; j < 5; ++j)
#pragma unroll
        for (int i = 0; i < 3; ++i) {
            unsigned so = (unsigned)(((5 * w + j) * 32 * F_ + ct * 48) * 2);
            bw[j][i] = __builtin_amdgcn_raw_buffer_load_b128(r1, vo[i], so, 0); bw[j][3 + i] = __builtin_amdgcn_raw_buffer_load_b128(r3, vo[i], so, 0);
        }
    {
        const int row = tid >> 3, c0 = tid & 7;
        v4u xa[10];
#pragma unroll
        for (int j = 0; j < 10; ++j) xa[j] = ld16c(rw, (unsigned)OFF_X1 + (unsigned)(((s * 32 + row) * D_ + (c0 + 8 * j) * 8) * 2));
        float sum = 0.f;
#pragma unroll
        for (int j = 0; j < 10; ++j) sum += bflo(xa[j].x) + bfhi(xa[j].x) + bflo(xa[j].y) + bfhi(xa[j].y) + bflo(xa[j].z) + bfhi(xa[j].z) + bflo(xa[j].w) + bfhi(xa[j].w);
        sum += __shfl_xor(sum, 1); sum += __shfl_xor(sum, 2); sum += __shfl_xor(sum, 4);
        const float mean = sum * (1.f / D_); float q = 0.f;
#pragma unroll
        for (int j = 0; j < 10; ++j) { float d;
            d = bflo(xa[j].x) - mean; q += d * d; d = bfhi(xa[j].x) - mean; q += d * d; d = bflo(xa[j].y) - mean; q += d * d; d = bfhi(xa[j].y) - mean; q += d * d;
            d = bflo(xa[j].z) - mean; q += d * d; d = bfhi(xa[j].z) - mean; q += d * d; d = bflo(xa[j].w) - mean; q += d * d; d = bfhi(xa[j].w) - mean; q += d * d; }
        q += __shfl_xor(q, 1); q += __shfl_xor(q, 2); q += __shfl_xor(q, 4);
        const float rstd = 1.f / sqrtf(q * (1.f / D_) + 1e-5f);
        const float* mod = (const float*)(ws + OFF_MOD);
#pragma unroll
        for (int j = 0; j < 10; ++j) {
            const int c = (c0 + 8 * j) * 8;
            f32x4 m0 = *(const f32x4*)(mod + 4 * 640 + c), m1 = *(const f32x4*)(mod + 4 * 640 + c + 4), s0 = *(const f32x4*)(mod + 3 * 640 + c), s1 = *(const f32x4*)(mod + 3 * 640 + c + 4);
            v4u u = xa[j], o;
            o.x = pk2((bflo(u.x) - mean) * rstd * (1.f + m0[0]) + s0[0], (bfhi(u.x) - mean) * rstd * (1.f + m0[1]) + s0[1]);
            o.y = pk2((bflo(u.y) - mean) * rstd * (1.f + m0[2]) + s0[2], (bfhi(u.y) - mean) * rstd * (1.f + m0[3]) + s0[3]);
            o.z = pk2((bflo(u.z) - mean) * rstd * (1.f + m1[0]) + s1[0], (bfhi(u.z) - mean) * rstd * (1.f + m1[1]) + s1[1]);
            o.w = pk2((bflo(u.w) - mean) * rstd * (1.f + m1[2]) + s1[2], (bfhi(u.w) - mean) * rstd * (1.f + m1[3]) + s1[3]);
            *(v4u*)(lds + row * F1_APITCH + (c0 + 8 * j) * 16) = o;
        }
    }
    __syncthreads();
    f32x4 acc[2][6];
#pragma unroll
    for (int a = 0; a < 2; ++a) for (int b = 0; b < 6; ++b) acc[a][b] = (f32x4){0.f, 0.f, 0.f, 0.f};
    char* bb = lds + F1_BOFF + w * 12288;
    int so2[6];
#pragma unroll
    for (int i = 0; i < 3; ++i) { int idx = lane + 64 * i, row = idx / 6, c = idx % 6, f = (row >> 3) & 1;
        so2[i] = row * 192 + (((c >> 1) ^ f) << 5) + ((c & 1) << 4); so2[3 + i] = row * 192 + ((((6 + c) >> 1) ^ f) << 5) + ((c & 1) << 4); }
    const int q4 = (lane & 15) >> 2, p4 = lane & 3;
#pragma unroll
    for (int j = 0; j < 5; ++j) {
        char* buf = bb + (j & 1) * 6144;
#pragma unroll
        for (int i = 0; i < 6; ++i) *(v4u*)(buf + so2[i]) = bw[j][i];
        bf16x8 af[2], bfr[6];
#pragma unroll
        for (int mt = 0; mt < 2; ++mt) af[mt] = *(const bf16x8*)(lds + (16 * mt + l15) * F1_APITCH + (4 * (5 * w + j) + g) * 16);
#pragma unroll
        for (int nt = 0; nt < 6; ++nt) { const int a = (8 * g + q4) * 192 + ((nt ^ (g & 1)) << 5) + 8 * p4; bfr[nt] = cat8(trread(buf, a), trread(buf, a + 4 * 192)); }
#pragma unroll
        for (int mt = 0; mt < 2; ++mt)
#pragma unroll
            for (int nt = 0; nt < 6; ++nt) acc[mt][nt] = mfma16(bfr[nt], af[mt], acc[mt][nt]);
    }
    __syncthreads();
    f32x4* red = (f32x4*)(lds + F1_ROFF);
#pragma unroll
    for (int mt = 0; mt < 2; ++mt)
#pragma unroll
        for (int nt = 0; nt < 6; ++nt) red[(w * 12 + mt * 6 + nt) * 64 + lane] = acc[mt][nt];
    __syncthreads();
    {
        const int mt = w & 1; f32x4 o[2];
#pragma unroll
        for (int n = 0; n < 2; ++n) {
            const int nt = (w < 2) ? n : 2;
            f32x4 ga = (red[(0 * 12 + mt * 6 + nt) * 64 + lane] + red[(1 * 12 + mt * 6 + nt) * 64 + lane]) + (red[(2 * 12 + mt * 6 + nt) * 64 + lane] + red[(3 * 12 + mt * 6 + nt) * 64 + lane]);
            f32x4 up = (red[(0 * 12 + mt * 6 + 3 + nt) * 64 + lane] + red[(1 * 12 + mt * 6 + 3 + nt) * 64 + lane]) + (red[(2 * 12 + mt * 6 + 3 + nt) * 64 + lane] + red[(3 * 12 + mt * 6 + 3 + nt) * 64 + lane]);
#pragma unroll
            for (int j = 0; j < 4; ++j) { float a = rbf(ga[j]), u = rbf(up[j]); float sl = rbf(a / (1.f + __expf(-a))); o[n][j] = sl * u; }
        }
        const int tok = s * 32 + 16 * mt + l15;
        v4u v = pair16(o[0], o[1]);
        if (w < 2) st16c(rw, (unsigned)OFF_ACT + (unsigned)((tok * F_ + ct * 48 + (g & 1) * 16 + 8 * (g >> 1)) * 2), v);
        else if ((g & 1) == 0) st16c(rw, (unsigned)OFF_ACT + (unsigned)((tok * F_ + ct * 48 + 32 + 8 * (g >> 1)) * 2), v);
    }
}

constexpr int F2_APITCH = 3088;
constexpr int F2_BOFF = 32 * F2_APITCH;
__device__ __forceinline__ void taskF2(char* lds, int s, int ct, const u16* __restrict__ w2, char* ws, u16* __restrict__ out, __amdgpu_buffer_rsrc_t rw) {
    int tid = threadIdx.x; asm volatile("" : "+v"(tid));
    const int lane = tid & 63, w = tid >> 6, g = lane >> 4, l15 = lane & 15;
    const __amdgpu_buffer_rsrc_t r2 = mkrs(w2, F_ * D_ * 2);
    v4u bw[12][4];
#pragma unroll
    for (int j = 0; j < 12; ++j)
#pragma unroll
        for (int i = 0; i < 4; ++i)
            bw[j][i] = __builtin_amdgcn_raw_buffer_load_b128(r2, (unsigned)((((lane >> 3) + 8 * i) * D_ + (lane & 7) * 8) * 2), (unsigned)(((12 * w + j) * 32 * D_ + ct * 64) * 2), 0);
    {
        const int row = tid >> 3, c0 = tid & 7;
#pragma unroll
        for (int j0 = 0; j0 < 24; j0 += 12) {
            v4u xa[12];
#pragma unroll
            for (int j = 0; j < 12; ++j) xa[j] = ld16c(rw, (unsigned)OFF_ACT + (unsigned)(((s * 32 + row) * F_ + (c0 + 8 * (j0 + j)) * 8) * 2));
#pragma unroll
            for (int j = 0; j < 12; ++j) *(v4u*)(lds + row * F2_APITCH + (c0 + 8 * (j0 + j)) * 16) = xa[j];
        }
    }
    __syncthreads();
    f32x4 acc[2][4];
#pragma unroll
    for (int a = 0; a < 2; ++a) for (int b = 0; b < 4; ++b) acc[a][b] = (f32x4){0.f, 0.f, 0.f, 0.f};
    char* bb = lds + F2_BOFF + w * 8192;
    int so2[4];
#pragma unroll
    for (int i = 0; i < 4; ++i) { int row = (lane >> 3) + 8 * i, c = lane & 7, f = ((row >> 1) & 1) | (((row >> 3) & 1) << 1); so2[i] = row * 128 + (((c >> 1) ^ f) << 5) + ((c & 1) << 4); }
    const int q4 = (lane & 15) >> 2, p4 = lane & 3, ff = ((q4 >> 1) & 1) | ((g & 1) << 1);
#pragma unroll
    for (int j = 0; j < 12; ++j) {
        char* buf = bb + (j & 1) * 4096;
#pragma unroll
        for (int i = 0; i < 4; ++i) *(v4u*)(buf + so2[i]) = bw[j][i];
        bf16x8 af[2], bfr[4];
#pragma unroll
        for (int mt = 0; mt < 2; ++mt) af[mt] = *(const bf16x8*)(lds + (16 * mt + l15) * F2_APITCH + (4 * (12 * w + j) + g) * 16);
#pragma unroll
        for (int nt = 0; nt < 4; ++nt) { const int a = (8 * g + q4) * 128 + ((nt ^ ff) << 5) + 8 * p4; bfr[nt] = cat8(trread(buf, a), trread(buf, a + 4 * 128)); }
#pragma unroll
        for (int mt = 0; mt < 2; ++mt)
#pragma unroll
            for (int nt = 0; nt < 4; ++nt) acc[mt][nt] = mfma16(bfr[nt], af[mt], acc[mt][nt]);
    }
    __syncthreads();
    f32x4* red = (f32x4*)lds;
#pragma unroll
    for (int mt = 0; mt < 2; ++mt)
#pragma unroll
        for (int nt = 0; nt < 4; ++nt) red[(w * 8 + mt * 4 + nt) * 64 + lane] = acc[mt][nt];
    __syncthreads();
    {
        const float* mod = (const float*)(ws + OFF_MOD);
        const int mt = w & 1, n0 = (w >> 1) * 2; f32x4 o[2];
        const int tok = s * 32 + 16 * mt + l15;
#pragma unroll
        for (int n = 0; n < 2; ++n) {
            const int T = mt * 4 + n0 + n;
            f32x4 v = (red[(0 * 8 + T) * 64 + lane] + red[(1 * 8 + T) * 64 + lane]) + (red[(2 * 8 + T) * 64 + lane] + red[(3 * 8 + T) * 64 + lane]);
            const int c = ct * 64 + (n0 + n) * 16 + 4 * g;
            v2u xv = ld8c(rw, (unsigned)OFF_X1 + (unsigned)((tok * D_ + c) * 2));
            float xs[4] = {bflo(xv.x), bfhi(xv.x), bflo(xv.y), bfhi(xv.y)};
#pragma unroll
            for (int j = 0; j < 4; ++j) o[n][j] = xs[j] + rbf(mod[5 * 640 + c + j] * rbf(v[j]));
        }
        *(v4u*)(out + (size_t)tok * D_ + ct * 64 + (n0 + (g & 1)) * 16 + 8 * (g >> 1)) = pair16(o[0], o[1]);
    }
}

constexpr int B2_ST = 2 * GBUF;
__device__ __forceinline__ void taskF1B(char* lds, int rbk, int ct, const u16* __restrict__ w1, const u16* __restrict__ w3, char* ws, __amdgpu_buffer_rsrc_t rw) {
    int tid = threadIdx.x; asm volatile("" : "+v"(tid));
    const int lane = tid & 63, w = tid >> 6, wm = w >> 1, wn = w & 1, g = lane >> 4, l15 = lane & 15;
    float2* stats = (float2*)(lds + B2_ST);
    { const float* mod = (const float*)(ws + OFF_MOD); float* ml = (float*)(lds + B2_ST + 1024);
      for (int i = tid; i < 640; i += 256) { ml[i] = 1.f + mod[4 * 640 + i]; ml[640 + i] = mod[3 * 640 + i]; } }
    const unsigned xb = (unsigned)OFF_X1 + (unsigned)(rbk * 128 * D_ * 2);
#pragma unroll 1
    for (int rr = 0; rr < 4; ++rr) {
        const int row = rr * 32 + (tid >> 3), c0 = tid & 7;
        v4u xa[10];
#pragma unroll
        for (int j = 0; j < 10; ++j) xa[j] = ld16c(rw, xb + (unsigned)((row * D_ + (c0 + 8 * j) * 8) * 2));
        float sum = 0.f;
#pragma unroll
        for (int j = 0; j < 10; ++j) sum += bflo(xa[j].x) + bfhi(xa[j].x) + bflo(xa[j].y) + bfhi(xa[j].y) + bflo(xa[j].z) + bfhi(xa[j].z) + bflo(xa[j].w) + bfhi(xa[j].w);
        sum += __shfl_xor(sum, 1); sum += __shfl_xor(sum, 2); sum += __shfl_xor(sum, 4);
        const float mean = sum * (1.f / D_); float q = 0.f;
#pragma unroll
        for (int j = 0; j < 10; ++j) { float d;
            d = bflo(xa[j].x) - mean; q += d * d; d = bfhi(xa[j].x) - mean; q += d * d; d = bflo(xa[j].y) - mean; q += d * d; d = bfhi(xa[j].y) - mean; q += d * d;
            d = bflo(xa[j].z) - mean; q += d * d; d = bfhi(xa[j].z) - mean; q += d * d; d = bflo(xa[j].w) - mean; q += d * d; d = bfhi(xa[j].w) - mean; q += d * d; }
        q += __shfl_xor(q, 1); q += __shfl_xor(q, 2); q += __shfl_xor(q, 4);
        if (c0 == 0) stats[row] = make_float2(mean, 1.f / sqrtf(q * (1.f / D_) + 1e-5f));
    }
    __syncthreads();
    const float* modl = (const float*)(lds + B2_ST + 1024);
    f32x4 acc[4][6];
    const int bt = tid < 192 ? tid : 0, brr = bt / 12, bc = bt % 12;
    const unsigned vb = (unsigned)((brr * F_ + (bc / 6) * 48 + (bc % 6) * 8) * 2);
    gemm4<10, PFD, 16>(lds, rw, (unsigned)(((tid >> 3) * D_ + (tid & 7) * 8) * 2), xb, D_ * 2,
          mkrs(w1, D_ * F_ * 2), mkrs(w3, D_ * F_ * 2), vb, vb, (unsigned)(ct * 96 * 2), F_ * 2, (bc / 6) * 12 + bc % 6, (bc / 6) * 12 + 6 + bc % 6,
          [&](int kc, int r, int ch, v4u u) {
              float2 st = stats[r]; const int c = kc * 64 + ch * 8;
              f32x4 m0 = *(const f32x4*)(modl + c), m1 = *(const f32x4*)(modl + c + 4), s0 = *(const f32x4*)(modl + 640 + c), s1 = *(const f32x4*)(modl + 640 + c + 4);
              v4u o;
              o.x = pk2((bflo(u.x) - st.x) * st.y * m0[0] + s0[0], (bfhi(u.x) - st.x) * st.y * m0[1] + s0[1]);
              o.y = pk2((bflo(u.y) - st.x) * st.y * m0[2] + s0[2], (bfhi(u.y) - st.x) * st.y * m0[3] + s0[3]);
              o.z = pk2((bflo(u.z) - st.x) * st.y * m1[0] + s1[0], (bfhi(u.z) - st.x) * st.y * m1[1] + s1[1]);
              o.w = pk2((bflo(u.w) - st.x) * st.y * m1[2] + s1[2], (bfhi(u.w) - st.x) * st.y * m1[3] + s1[3]);
              return o; }, acc);
#pragma unroll
    for (int mt = 0; mt < 4; ++mt) {
        f32x4 o[4];
#pragma unroll
        for (int nt = 0; nt < 3; ++nt)
#pragma unroll
            for (int j = 0; j < 4; ++j) { float ga = rbf(acc[mt][nt][j]), up = rbf(acc[mt][nt + 3][j]); float sl = rbf(ga / (1.f + __expf(-ga))); o[nt][j] = sl * up; }
        int tok = rbk * 128 + wm * 64 + mt * 16 + l15;
        int f0 = ct * 96 + wn * 48 + (g & 1) * 16 + 8 * (g >> 1);
        st16c(rw, (unsigned)OFF_ACT + (unsigned)((tok * F_ + f0) * 2), pair16(o[0], o[1]));
        v4u y = pair16(o[2], o[2]);
        int f1 = ct * 96 + wn * 48 + 32 + 8 * (g >> 1);
        if ((g & 1) == 0) st16c(rw, (unsigned)OFF_ACT + (unsigned)((tok * F_ + f1) * 2), y);
    }
}
__device__ __forceinline__ void taskF2B(char* lds, int rbk, int ct, const u16* __restrict__ w2, char* ws, u16* __restrict__ out, __amdgpu_buffer_rsrc_t rw) {
    int tid = threadIdx.x; asm volatile("" : "+v"(tid));
    const int lane = tid & 63, w = tid >> 6, wm = w >> 1, wn = w & 1, g = lane >> 4, l15 = lane & 15;
    f32x4 acc[4][6];
    const unsigned ab = (unsigned)OFF_ACT + (unsigned)(rbk * 128 * F_ * 2);
    const int bt = tid < 192 ? tid : 0, brr = bt / 12, bc = bt % 12;
    const __amdgpu_buffer_rsrc_t rW = mkrs(w2, F_ * D_ * 2);
    int c0 = ct * 192 + bc * 8, c1 = ct * 192 + (12 + bc) * 8; if (c0 >= D_) c0 = 0; if (c1 >= D_) c1 = 0;
    gemm4<24, PFD2, 16>(lds, rw, (unsigned)(((tid >> 3) * F_ + (tid & 7) * 8) * 2), ab, F_ * 2,
          rW, rW, (unsigned)((brr * D_ + c0) * 2), (unsigned)((brr * D_ + c1) * 2), 0u, D_ * 2, bc, 12 + bc, [](int, int, int, v4u v) { return v; }, acc);
    const float* mod = (const float*)(ws + OFF_MOD);
#pragma unroll
    for (int mt = 0; mt < 4; ++mt)
#pragma unroll
        for (int nt = 0; nt < 6; ++nt) {
            int tok = rbk * 128 + wm * 64 + mt * 16 + l15, c = ct * 192 + wn * 96 + nt * 16 + 4 * g;
            if (c < D_) {
                v2u xv = ld8c(rw, (unsigned)OFF_X1 + (unsigned)((tok * D_ + c) * 2));
                float xs[4] = {bflo(xv.x), bfhi(xv.x), bflo(xv.y), bfhi(xv.y)}; float o[4];
#pragma unroll
                for (int j = 0; j < 4; ++j) o[j] = xs[j] + rbf(mod[5 * 640 + c + j] * rbf(acc[mt][nt][j]));
                *(v2u*)(out + (size_t)tok * D_ + c) = (v2u){pk2(o[0], o[1]), pk2(o[2], o[3])};
            }
        }
}

#define REDMODE 1
constexpr int C_RES = 0;
constexpr int C_MEM = 122880;
constexpr int C_SP  = 133120;
constexpr int C_FLAG = 145408;
constexpr int C_LDS = 145408 + 64;

__device__ __forceinline__ int lds_flag_get(char* lds) { return *(volatile LAS int*)(LAS char*)(lds + C_FLAG); }
__device__ __forceinline__ void lds_flag_set(char* lds, int v) { *(volatile LAS int*)(LAS char*)(lds + C_FLAG) = v; }
__device__ __forceinline__ bool poll_mem(__amdgpu_buffer_rsrc_t rs, unsigned goff, char* ldst, int tid) {
    v4u v[3]; unsigned spins = 0; bool dead = false;
    while (true) {
        asm volatile("" ::: "memory");
        v[0] = ld16c(rs, goff + tid * 16); v[1] = ld16c(rs, goff + (tid + 256) * 16);
        if (tid < 128) v[2] = ld16c(rs, goff + (tid + 512) * 16); else v[2] = v[1];
        bool ok = okv(v[0]) && okv(v[1]) && okv(v[2]);
        if (ok) break;
        if (++spins > kSpinCap) { dead = true; break; }
    }
    *(v4u*)(ldst + tid * 16) = v[0]; *(v4u*)(ldst + (tid + 256) * 16) = v[1];
    if (tid < 128) *(v4u*)(ldst + (tid + 512) * 16) = v[2];
    return dead;
}

template <int NR, int BT> __device__ __forceinline__ void stage192(char* ldst, const u16* __restrict__ gsrc, int tid) {
    if (tid < 192) {
        const int r0 = tid / 24, ch = tid % 24;
        const u16* gp = gsrc + (size_t)r0 * HE + ch * 8;
        const int o0 = r0 * 384 + (((ch >> 1) ^ ((r0 >> 1) & 1)) << 5) + ((ch & 1) << 4);
        const int o1 = r0 * 384 + (((ch >> 1) ^ (((r0 >> 1) & 1) | 2)) << 5) + ((ch & 1) << 4);
#pragma unroll 1
        for (int i0 = 0; i0 < NR / 8; i0 += BT) {
            v4u r[BT];
#pragma unroll
            for (int i = 0; i < BT; ++i) r[i] = *(const v4u*)(gp + (size_t)(i0 + i) * 8 * HE);
#pragma unroll
            for (int i = 0; i < BT; ++i) *(v4u*)(ldst + ((i & 1) ? o1 : o0) + (i0 + i) * 3072) = r[i];
        }
    }
}
__device__ __forceinline__ void stage640(char* ldst, const u16* __restrict__ gsrc, int tid) {
    const int r0 = tid >> 4, c0 = tid & 15;
    const int f = (r0 & 3) | (((r0 >> 3) & 1) << 2);
    const u16* gp = gsrc + (size_t)r0 * D_ + c0 * 8;
    const int o = r0 * 1280 + (((c0 >> 1) ^ f) << 5) + ((c0 & 1) << 4);
#pragma unroll 1
    for (int i = 0; i < 6; ++i) {
        v4u r[5];
#pragma unroll
        for (int j = 0; j < 5; ++j) r[j] = *(const v4u*)(gp + (size_t)i * 16 * D_ + j * 128);
#pragma unroll
        for (int j = 0; j < 5; ++j) *(v4u*)(ldst + o + i * 16 * 1280 + j * 256) = r[j];
    }
}

__device__ __forceinline__ void chain_qk(char* lds, int h, const u16* __restrict__ Wq, const u16* __restrict__ Wk, char* ws, __amdgpu_buffer_rsrc_t rs, __amdgpu_buffer_rsrc_t rw) {
    gu32* ctl = (gu32*)(ws + OFF_CTL);
    const int tid = threadIdx.x, lane = tid & 63, w = tid >> 6, g = lane >> 4, l15 = lane & 15;
    const int et0 = 3 * w;
    bf16x8 wq[3][15], wk[3][15];
#pragma unroll
    for (int c = 0; c < 3; ++c) {
        __syncthreads();
        stage192<160, 10>(lds, Wq + (size_t)(c * 160) * HE + h * E_, tid); stage192<160, 10>(lds + 61440, Wk + (size_t)(c * 160) * HE + h * E_, tid);
        __syncthreads();
#pragma unroll
        for (int k5 = 0; k5 < 5; ++k5)
#pragma unroll
            for (int j = 0; j < 3; ++j) { wq[j][c * 5 + k5] = trfrag192(lds, 32 * k5, et0 + j, lane); wk[j][c * 5 + k5] = trfrag192(lds + 61440, 32 * k5, et0 + j, lane); }
    }
    __syncthreads();
    stage192<160, 4>(lds + C_RES, Wq + (size_t)480 * HE + h * E_, tid); stage192<160, 4>(lds + C_RES + 61440, Wk + (size_t)480 * HE + h * E_, tid);
    const unsigned kg0 = (unsigned)OFF_K + (unsigned)((l15 * HE + h * E_ + 48 * w + 4 * g) * 2);
    v2u kx[2][3];
    wait_cnt(ctl, CW_A, 32);
#pragma unroll
    for (int kt = 0; kt < 2; ++kt)
#pragma unroll
        for (int j = 0; j < 3; ++j) kx[kt][j] = ld8c(rw, kg0 + (unsigned)((16 * kt) * HE * 2 + 32 * j));
    if (tid == 0) lds_flag_set(lds, 0);
    __syncthreads();
    bf16x8 la[6];
#pragma unroll
    for (int j = 0; j < 3; ++j) { la[2 * j] = trfrag192(lds + C_RES, 0, et0 + j, lane); la[2 * j + 1] = trfrag192(lds + C_RES + 61440, 0, et0 + j, lane); }
    const float scale = 0.07216878364870322f;
#pragma unroll 1
    for (int s = 0; s < NSEG; ++s) {
        ST(h, s, 0);
        bool dead = poll_mem(rs, s * 10240, lds + C_MEM, tid);
        if (dead) lds_flag_set(lds, 1);
        bar_lds();
        if (lds_flag_get(lds)) break;
        ST(h, s, 1);
        f32x4 aq[3], ak[3];
#pragma unroll
        for (int j = 0; j < 3; ++j) { aq[j] = (f32x4){0.f, 0.f, 0.f, 0.f}; ak[j] = (f32x4){0.f, 0.f, 0.f, 0.f}; }
        {
            const char* mb = lds + C_MEM + (g * 8 + (lane & 7)) * 16;
            bf16x8 b[20];
            b[0] = *(const bf16x8*)(mb + 0 * 512); b[1] = *(const bf16x8*)(mb + 1 * 512);
#pragma unroll
            for (int p = 0; p < 20; ++p) {
                const int ks = ((p & 3) == 3) ? 15 + (p >> 2) : 3 * (p >> 2) + (p & 3);
                if (p + 2 < 20) { const int k2 = (((p + 2) & 3) == 3) ? 15 + ((p + 2) >> 2) : 3 * ((p + 2) >> 2) + ((p + 2) & 3); b[p + 2] = *(const bf16x8*)(mb + k2 * 512); }
#pragma unroll
                for (int j = 0; j < 3; ++j) {
                    bf16x8 a0 = ((p & 3) != 3) ? wq[j][ks < 15 ? ks : 0] : la[2 * j];
                    aq[j] = mfma16(a0, b[p], aq[j]);
                    bf16x8 a1 = ((p & 3) != 3) ? wk[j][ks < 15 ? ks : 0] : la[2 * j + 1];
                    ak[j] = mfma16(a1, b[p], ak[j]);
                }
                SB();
                if ((p & 3) == 3) {
#pragma unroll
                    for (int j = 0; j < 3; ++j) { la[2 * j] = trfrag192(lds + C_RES, 32 * (((p >> 2) + 1) % 5), et0 + j, lane); la[2 * j + 1] = trfrag192(lds + C_RES + 61440, 32 * (((p >> 2) + 1) % 5), et0 + j, lane); }
                    SB();
                }
            }
        }
        v2u qp[3], kp[3];
#pragma unroll
        for (int j = 0; j < 3; ++j) { qp[j] = (v2u){pk2(aq[j][0], aq[j][1]), pk2(aq[j][2], aq[j][3])}; kp[j] = (v2u){pk2(ak[j][0], ak[j][1]), pk2(ak[j][2], ak[j][3])}; }
        if (s + 1 < NSEG) {
            bf16x8 q0 = __builtin_bit_cast(bf16x8, (v4u){qp[0].x, qp[0].y, qp[1].x, qp[1].y}), q1 = __builtin_bit_cast(bf16x8, (v4u){qp[2].x, qp[2].y, 0u, 0u});
            bf16x8 k0 = __builtin_bit_cast(bf16x8, (v4u){kp[0].x, kp[0].y, kp[1].x, kp[1].y}), k1 = __builtin_bit_cast(bf16x8, (v4u){kp[2].x, kp[2].y, 0u, 0u});
            f32x4 z = {0.f, 0.f, 0.f, 0.f};
            f32x4 s0 = mfma16(k1, q1, mfma16(k0, q0, z));
            f32x4 sx[2];
#pragma unroll
            for (int kt = 0; kt < 2; ++kt) {
                bf16x8 x0 = __builtin_bit_cast(bf16x8, (v4u){kx[kt][0].x, kx[kt][0].y, kx[kt][1].x, kx[kt][1].y}), x1 = __builtin_bit_cast(bf16x8, (v4u){kx[kt][2].x, kx[kt][2].y, 0u, 0u});
                sx[kt] = mfma16(x1, q1, mfma16(x0, q0, z));
            }
            f32x4* sp = (f32x4*)(lds + C_SP) + w * 192 + lane;
            sp[0] = s0; sp[64] = sx[0]; sp[128] = sx[1];
        }
        bar_lds();
        ST(h, s, 2);
        if (w == 0 && s + 1 < NSEG) {
            const f32x4* sp = (const f32x4*)(lds + C_SP) + lane;
            f32x4 sc[3];
#pragma unroll
            for (int a = 0; a < 3; ++a) sc[a] = (sp[a * 64] + sp[192 + a * 64]) + (sp[384 + a * 64] + sp[576 + a * 64]);
            const float sl2 = scale * 1.4426950408889634f;
            float mx = -3.0e38f;
#pragma unroll
            for (int r = 0; r < 4; ++r) { float t0 = sc[0][r] * sl2; sc[0][r] = (g < 2) ? t0 : -3.0e38f; sc[1][r] *= sl2; sc[2][r] *= sl2;
                mx = fmaxf(mx, fmaxf(sc[0][r], fmaxf(sc[1][r], sc[2][r]))); }
            mx = xmax32(xmax16(mx));
            float sum = 0.f;
#pragma unroll
            for (int r = 0; r < 4; ++r) { sc[0][r] = __builtin_amdgcn_exp2f(sc[0][r] - mx); sc[1][r] = __builtin_amdgcn_exp2f(sc[1][r] - mx); sc[2][r] = __builtin_amdgcn_exp2f(sc[2][r] - mx); sum += sc[0][r] + sc[1][r] + sc[2][r]; }
            sum = xsum32(xsum16(sum));
            const float inv = __builtin_amdgcn_rcpf(sum);
            {
                unsigned a0 = pk2(sc[0][0] * inv, sc[0][1] * inv), a1 = pk2(sc[0][2] * inv, sc[0][3] * inv);
                unsigned b0 = pk2(sc[1][0] * inv, sc[1][1] * inv), b1 = pk2(sc[1][2] * inv, sc[1][3] * inv);
                unsigned c0 = pk2(sc[2][0] * inv, sc[2][1] * inv), c1 = pk2(sc[2][2] * inv, sc[2][3] * inv);
                auto x0 = __builtin_amdgcn_permlane16_swap(b0, c0, false, false); auto x1 = __builtin_amdgcn_permlane16_swap(b1, c1, false, false);
                auto y0 = __builtin_amdgcn_permlane16_swap(a0, a0, false, false); auto y1 = __builtin_amdgcn_permlane16_swap(a1, a1, false, false);
                const unsigned pb = (unsigned)(OFF_P - OFF_MEM) + s * 20480 + h * 640 + l15 * 16;
                if (l15 < 8) {
                    st16c(rs, pb + (1 + (g >> 1) + 2 * (g & 1)) * 128, (v4u){x0[0], x1[0], x0[1], x1[1]});
                    if (g == 0) st16c(rs, pb, (v4u){y0[0], y1[0], y0[1], y1[1]});
                }
            }
        }
        ST(h, s, 3);
        {
            auto x0 = __builtin_amdgcn_permlane16_swap(kp[0].x, kp[1].x, false, false); auto x1 = __builtin_amdgcn_permlane16_swap(kp[0].y, kp[1].y, false, false);
            auto y0 = __builtin_amdgcn_permlane16_swap(kp[2].x, kp[2].x, false, false); auto y1 = __builtin_amdgcn_permlane16_swap(kp[2].y, kp[2].y, false, false);
            const unsigned kb = (unsigned)OFF_KM + (unsigned)((((s * 32 + h) * 8 + l15) * E_ + 48 * w + 8 * (g >> 1)) * 2);
            if (l15 < 8) {
                st16c(rw, kb + 32 * (g & 1), (v4u){x0[0], x1[0], x0[1], x1[1]});
                if ((g & 1) == 0) st16c(rw, kb + 64, (v4u){y0[0], y1[0], y0[1], y1[1]});
            }
        }
        if (s + 1 < NSEG) {
            if (((s + 1) & 3) == 0) wait_cnt(ctl, CW_A + 32 * ((s + 1) >> 2), 32);
#pragma unroll
            for (int kt = 0; kt < 2; ++kt)
#pragma unroll
                for (int j = 0; j < 3; ++j) kx[kt][j] = ld8c(rw, kg0 + (unsigned)(((s + 1) * 32 + 16 * kt) * HE * 2 + 32 * j));
        }
    }
}

__device__ __forceinline__ void chain_vo(char* lds, int h, const u16* __restrict__ Wv, const u16* __restrict__ Wo, char* ws, __amdgpu_buffer_rsrc_t rs) {
    const int tid = threadIdx.x, lane = tid & 63, w = tid >> 6, g = lane >> 4, l15 = lane & 15;
    const int et0 = 3 * w;
    bf16x8 wv[3][20], wo[10][3];
#pragma unroll
    for (int c = 0; c < 2; ++c) {
        __syncthreads();
        stage192<320, 10>(lds, Wv + (size_t)(c * 320) * HE + h * E_, tid);
        __syncthreads();
#pragma unroll
        for (int k = 0; k < 10; ++k)
#pragma unroll
            for (int j = 0; j < 3; ++j) wv[j][c * 10 + k] = trfrag192(lds, 32 * k, et0 + j, lane);
    }
    const u16* Woh = Wo + (size_t)h * E_ * D_;
    __syncthreads();
    stage640(lds, Woh, tid);
    __syncthreads();
#pragma unroll
    for (int n = 0; n < 10; ++n)
#pragma unroll
        for (int t = 0; t < 3; ++t) wo[n][t] = trfragP<1280>(lds, 32 * t, 10 * w + n, lane);
    __syncthreads();
    stage640(lds + C_RES, Woh + 96 * D_, tid);
    if (tid == 0) lds_flag_set(lds, 0);
    __syncthreads();
    bf16x8 lb[2][5];
#pragma unroll
    for (int n = 0; n < 5; ++n) { lb[0][n] = trfragP<1280>(lds + C_RES, 0, 10 * w + n, lane); lb[1][n] = trfragP<1280>(lds + C_RES, 0, 10 * w + 5 + n, lane); }
    char* vfr = lds + C_SP;
#pragma unroll 1
    for (int s = 0; s < NSEG; ++s) {
        ST(32 + h, s, 0);
        bool dead = poll_mem(rs, s * 10240, lds + C_MEM, tid);
        if (dead) lds_flag_set(lds, 1);
        bar_lds();
        if (lds_flag_get(lds)) break;
        ST(32 + h, s, 1);
        f32x4 acc[3] = {{0.f, 0.f, 0.f, 0.f}, {0.f, 0.f, 0.f, 0.f}, {0.f, 0.f, 0.f, 0.f}};
        {
            const char* mb = lds + C_MEM + (g * 8 + (lane & 7)) * 16;
            bf16x8 b[20];
#pragma unroll
            for (int p = 0; p < 4; ++p) b[p] = *(const bf16x8*)(mb + p * 512);
#pragma unroll
            for (int p = 0; p < 20; ++p) {
                if (p + 4 < 20) b[p + 4] = *(const bf16x8*)(mb + (p + 4) * 512);
#pragma unroll
                for (int j = 0; j < 3; ++j) acc[j] = mfma16(wv[j][p], b[p], acc[j]);
                SB();
            }
        }
        if (l15 < 8) {
#pragma unroll
            for (int j = 0; j < 3; ++j) *(v2u*)(vfr + ((6 * w + 2 * j + (g >> 1)) * 8 + l15) * 16 + (g & 1) * 8) = (v2u){pk2(acc[j][0], acc[j][1]), pk2(acc[j][2], acc[j][3])};
        }
        bar_lds();
        ST(32 + h, s, 2);
        f32x4 ua[10];
#pragma unroll
        for (int n = 0; n < 10; ++n) ua[n] = (f32x4){0.f, 0.f, 0.f, 0.f};
        {
            const char* vb = vfr + (g * 8 + (lane & 7)) * 16;
            bf16x8 a[6];
#pragma unroll
            for (int t = 0; t < 6; ++t) a[t] = *(const bf16x8*)(vb + t * 512);
#pragma unroll
            for (int k = 0; k < 6; ++k) {
#pragma unroll
                for (int n = 0; n < 5; ++n) ua[5 * (k & 1) + n] = mfma16(a[k >> 1], wo[5 * (k & 1) + n][k >> 1], ua[5 * (k & 1) + n]);
                SB();
#pragma unroll
                for (int n = 0; n < 5; ++n) ua[5 * (k & 1) + n] = mfma16(a[3 + (k >> 1)], lb[k & 1][n], ua[5 * (k & 1) + n]);
                SB();
#pragma unroll
                for (int n = 0; n < 5; ++n) lb[k & 1][n] = trfragP<1280>(lds + C_RES, 32 * (((k + 2) % 6) >> 1), 10 * w + 5 * (k & 1) + n, lane);
                SB();
            }
        }
        {
#pragma unroll
            for (int n = 0; n < 10; n += 2) {
                unsigned a0 = pk2(ua[n][0], ua[n][1]), a1 = pk2(ua[n][2], ua[n][3]), b0 = pk2(ua[n + 1][0], ua[n + 1][1]), b1 = pk2(ua[n + 1][2], ua[n + 1][3]);
                auto x0 = __builtin_amdgcn_permlane16_swap(a0, b0, false, false); auto x1 = __builtin_amdgcn_permlane16_swap(a1, b1, false, false);
                unsigned off = (unsigned)(OFF_UM - OFF_MEM) + ((s * 32 + h) * 40 + 10 * w + n + g) * 256 + l15 * 16;
                if (g < 2) st16c(rs, off, (v4u){x0[0], x1[0], x0[1], x1[1]});
            }
        }
        ST(32 + h, s, 3);
    }
}

__device__ __forceinline__ void chain_red(char* lds, int r, char* ws, __amdgpu_buffer_rsrc_t rs, __amdgpu_buffer_rsrc_t rw) {
    gu32* ctl = (gu32*)(ws + OFF_CTL);
    const int tid = threadIdx.x, lane = tid & 63, w = tid >> 6, g = lane >> 4, l15 = lane & 15;
    float* red = (float*)lds;
    if (tid == 0) lds_flag_set(lds, 0);
    __syncthreads();
    auto ldux = [&](int s, v4u (&ux)[10]) {
#pragma unroll
        for (int t = 0; t < 10; ++t) { int cgl = 4 * t + g, hh = cgl / 5, jg = cgl % 5; int jj = jg ? jg - 1 : 0;
            ux[t] = ld16c(rw, (unsigned)OFF_UX + (unsigned)(((((s * 32 + 8 * w + hh) * 40 + r) * 4 + jj) * 16 + l15) * 16)); }
    };
    v4u ux[10]; wait_cnt(ctl, CW_A, 32); ldux(0, ux);
#pragma unroll 1
    for (int s = 0; s + 1 < NSEG; ++s) {
        v4u pf[10], um0, um1; unsigned spins = 0; bool dead = false;
        ST(64 + r, s, 0);
        const unsigned pb = (unsigned)(OFF_P - OFF_MEM) + s * 20480 + ((40 * w + g) * 8 + (lane & 7)) * 16;
        const unsigned ub = (unsigned)(OFF_UM - OFF_MEM) + ((s * 32 + 8 * w + g) * 40 + r) * 256 + l15 * 16;
        while (true) {
            asm volatile("" ::: "memory");
            um0 = ld16c(rs, ub); um1 = ld16c(rs, ub + 4 * 40 * 256);
            if (__all(okv(um0) && okv(um1))) break;
            if (++spins > kSpinCap) { dead = true; break; }
        }
        {
            const unsigned cb = (unsigned)(OFF_P - OFF_MEM) + s * 20480 + (8 * w + (lane >> 3)) * 640 + 4 * 128 + (lane & 7) * 16;
            while (true) {
                asm volatile("" ::: "memory");
                v4u c = ld16c(rs, cb);
                if (__all(okv(c))) break;
                if (++spins > kSpinCap) { dead = true; break; }
            }
        }
        while (true) {
            asm volatile("" ::: "memory");
#pragma unroll
            for (int t = 0; t < 10; ++t) pf[t] = ld16c(rs, pb + t * 512);
            bool ok = true;
#pragma unroll
            for (int t = 0; t < 10; ++t) ok &= okv(pf[t]);
            if (__all(ok)) break;
            if (++spins > kSpinCap) { dead = true; break; }
        }
        ST(64 + r, s, 1);
        f32x4 acc0 = {0.f, 0.f, 0.f, 0.f}, acc1 = {0.f, 0.f, 0.f, 0.f};
#pragma unroll
        for (int t = 0; t < 5; ++t) {
            v4u a = (t < 4 && g == t) ? um0 : ux[t];
            acc0 = mfma16(__builtin_bit_cast(bf16x8, a), __builtin_bit_cast(bf16x8, pf[t]), acc0);
            v4u b = (t < 4 && g == t) ? um1 : ux[5 + t];
            acc1 = mfma16(__builtin_bit_cast(bf16x8, b), __builtin_bit_cast(bf16x8, pf[5 + t]), acc1);
        }
        acc0 += acc1;
        if (l15 < 8) *(f32x4*)(red + (w * 8 + l15) * 16 + 4 * g) = acc0;
        if (dead) lds_flag_set(lds, 1);
        bar_lds();
        if (lds_flag_get(lds)) break;
        ST(64 + r, s, 2);
        if (w == 0) {
            int i = lane >> 3, cp = lane & 7; float a0 = 0.f, a1 = 0.f;
#pragma unroll
            for (int q = 0; q < 4; ++q) { float2 p = *(const float2*)(red + (q * 8 + i) * 16 + 2 * cp); a0 += p.x; a1 += p.y; }
            unsigned d0 = pk2(a0, a1);
            unsigned d1 = __builtin_amdgcn_mov_dpp(d0, 0x55, 0xf, 0xf, true), d2 = __builtin_amdgcn_mov_dpp(d0, 0xAA, 0xf, 0xf, true), d3 = __builtin_amdgcn_mov_dpp(d0, 0xFF, 0xf, 0xf, true);
            if ((cp & 3) == 0) st16c(rs, (s + 1) * 10240 + r * 256 + (cp >> 2) * 128 + i * 16, (v4u){d0, d1, d2, d3});
        }
        ST(64 + r, s, 3);
        if (s + 2 < NSEG) { if (((s + 1) & 3) == 0) wait_cnt(ctl, CW_A + 32 * ((s + 1) >> 2), 32); ldux(s + 1, ux); }
        bar_lds();
    }
}

struct Params { const u16 *x, *Wq, *Wk, *Wv, *Wo, *w1, *w3, *w2; char* ws; u16* out; };
__global__ void __launch_bounds__(256, 1) k_main(Params p) {
    extern __shared__ __attribute__((aligned(16))) char lds[];
    const int b = blockIdx.x, tid = threadIdx.x;
    char* ws = p.ws;
    const __amdgpu_buffer_rsrc_t rs = __builtin_amdgcn_make_buffer_rsrc(ws + OFF_MEM, 0, (int)(OFF_PT - OFF_MEM), 0x00020000);
    const __amdgpu_buffer_rsrc_t rw = __builtin_amdgcn_make_buffer_rsrc(ws, 0, (int)OFF_END, 0x00020000);
    if (b < 32) { chain_qk(lds, b, p.Wq, p.Wk, ws, rs, rw); return; }
    if (b < 64) { chain_vo(lds, b - 32, p.Wv, p.Wo, ws, rs); return; }
    if (b < 104) { chain_red(lds, b - 64, ws, rs, rw); return; }
    gu32* ctl = (gu32*)(ws + OFF_CTL);
    const int* tab = (const int*)(ws + OFF_TAB);
    volatile LAS int* tslot = (volatile LAS int*)(LAS char*)(lds + C_FLAG);
#pragma unroll 1
    while (true) {
        __syncthreads();
        if (tid == 0) { int t = (int)__hip_atomic_fetch_add(ctl + CW_QUEUE, 1u, RLX_AGENT); tslot[0] = t; tslot[1] = t < NTASK ? tab[t] : -1; }
        __syncthreads();
        const int t = __builtin_amdgcn_readfirstlane(tslot[0]);
        if (t >= NTASK) break;
        const int code = __builtin_amdgcn_readfirstlane(tslot[1]), ty = code >> 16, ta = (code >> 8) & 255, tb = code & 255;
        int sig = -1;
#define TS1() do {} while (0)
        if (ty == 0) {
            TS1(); taskA(lds, ta, tb, 0, p.Wq, p.Wk, p.Wv, p.Wo, ws, rw); sig = CW_A + 32 * ta;
        } else if (ty == 1) {
            TS1(); taskA(lds, ta, tb, 1, p.Wq, p.Wk, p.Wv, p.Wo, ws, rw); sig = CW_Q + 32 * ta;
        } else if (ty == 2) {
            if (tid < 64) { wait_cnt(ctl, CW_A + 32 * (ta >> 2), 32); wait_cnt(ctl, CW_Q + 32 * (ta >> 2), 32); }
            __syncthreads();
            TS1(); taskP(ta, tb, ws, rw); sig = CW_P + 32 * ta;
        } else if (ty == 3) {
            if (tid < 64) wait_cnt(ctl, CW_P + 32 * ta, 4);
            __syncthreads();
            TS1(); taskO(ta, tb, p.x, ws, rw); sig = CW_X + 32 * ta;
        } else if (ty == 4) {
            if (tid < 64) wait_cnt(ctl, CW_X + 32 * ta, 5);
            __syncthreads();
            TS1(); taskF1(lds, ta, tb, p.w1, p.w3, ws, rw); sig = CW_F + 32 * ta;
        } else if (ty == 5) {
            if (tid < 64) wait_cnt(ctl, CW_F + 32 * ta, 32);
            __syncthreads();
            TS1(); taskF2(lds, ta, tb, p.w2, ws, p.out, rw);
        } else if (ty == 6) {
            if (tid < 64) { for (int j = 0; j < 4; ++j) wait_cnt(ctl, CW_X + 32 * (4 * ta + j), 5); }
            __syncthreads();
            TS1(); taskF1B(lds, ta, tb, p.w1, p.w3, ws, rw); sig = CW_F + 32 * (4 * ta);
        } else {
            if (tid < 64) wait_cnt(ctl, CW_F + 32 * (4 * ta), 16);
            __syncthreads();
            TS1(); taskF2B(lds, ta, tb, p.w2, ws, p.out, rw);
        }
        drain_vm();
        __syncthreads();
        if (tid == 0 && sig >= 0) __hip_atomic_fetch_add(ctl + sig, 1u, RLX_AGENT);
    }
}

extern "C" void kernel_launch(void* const* d_in, const int* in_sizes, int n_in, void* d_out, int out_size, void* d_ws, size_t ws_size, hipStream_t stream) {
    const u16* x = (const u16*)d_in[0]; const u16* mem0 = (const u16*)d_in[1];
    const u16* c = (const u16*)d_in[11]; const u16* wmod = (const u16*)d_in[12];
    char* ws = (char*)d_ws;
    Params p; p.x = x; p.Wq = (const u16*)d_in[2]; p.Wk = (const u16*)d_in[3]; p.Wv = (const u16*)d_in[4]; p.Wo = (const u16*)d_in[5];
    p.w1 = (const u16*)d_in[8]; p.w3 = (const u16*)d_in[9]; p.w2 = (const u16*)d_in[10]; p.ws = ws; p.out = (u16*)d_out;
    (void)hipFuncSetAttribute((const void*)k_main, hipFuncAttributeMaxDynamicSharedMemorySize, C_LDS);
    k_pre<<<256, 256, 0, stream>>>(c, wmod, mem0, ws);
    k_ln<<<S_ / 4, 256, 0, stream>>>(x, (const float*)(ws + OFF_MOD), 0, 640, (u16*)(ws + OFF_H));
    k_main<<<256, 256, C_LDS, stream>>>(p);
}
```

```cpp
#include <hip/hip_runtime.h>
#include <hip/hip_bf16.h>
#include <stdint.h>

#define LAS __attribute__((address_space(3)))
#define GAS __attribute__((address_space(1)))
typedef short bf16x8 __attribute__((ext_vector_type(8)));
typedef short bf16x4 __attribute__((ext_vector_type(4)));
typedef float f32x4 __attribute__((ext_vector_type(4)));
typedef unsigned v4u __attribute__((ext_vector_type(4)));
typedef unsigned v2u __attribute__((ext_vector_type(2)));
typedef unsigned short u16;
typedef GAS unsigned gu32;
#define RLX_AGENT __ATOMIC_RELAXED, __HIP_MEMORY_SCOPE_AGENT

constexpr int S_ = 2048, D_ = 640, H_ = 32, E_ = 192, HE = 6144, NSEG = 64, F_ = 1536;
constexpr size_t OFF_MOD = 0;
constexpr size_t OFF_CTL = 16384;
constexpr size_t OFF_TAB = 49152;
constexpr size_t OFF_H   = 65536;
constexpr size_t OFF_Q   = OFF_H + (size_t)S_ * D_ * 2;
constexpr size_t OFF_K   = OFF_Q + (size_t)S_ * HE * 2;
constexpr size_t OFF_UX  = OFF_K + (size_t)S_ * HE * 2;
constexpr size_t OFF_MEM = OFF_UX + (size_t)NSEG * H_ * 40 * 4 * 256;
constexpr size_t OFF_P   = OFF_MEM + (size_t)65 * 10240;
constexpr size_t OFF_UM  = OFF_P + (size_t)NSEG * 20480;
constexpr size_t OFF_KM  = OFF_UM + (size_t)NSEG * H_ * 40 * 256;
constexpr size_t OFF_PT  = OFF_KM + (size_t)NSEG * H_ * 8 * E_ * 2;
constexpr size_t OFF_X1  = OFF_PT + (size_t)S_ * 1280 * 2;
constexpr size_t OFF_ACT = OFF_X1 + (size_t)S_ * D_ * 2;
constexpr size_t OFF_END = OFF_ACT + (size_t)S_ * F_ * 2;
constexpr unsigned SENT = 0xFFFFFFFFu;
constexpr unsigned kSpinCap = 1u << 20;
constexpr int CW_QUEUE = 0, CW_TMO = 32, CW_A = 64  , CW_Q = CW_A + 32 * 16  , CW_P = CW_Q + 32 * 16  , CW_X = CW_P + 32 * 64, CW_F = CW_X + 32 * 64, CW_FILL = CW_F + 32 * 64, CW_LN = CW_FILL + 32, CW_END = CW_LN + 32 * 16;
static_assert(CW_END * 4 <= 32768, "ctl");
#define ST(role_idx, s_, k_) do {} while (0)

__device__ __forceinline__ float bf2f(u16 v) { return __uint_as_float((unsigned)v << 16); }
__device__ __forceinline__ float bflo(unsigned v) { return __uint_as_float(v << 16); }
__device__ __forceinline__ float bfhi(unsigned v) { return __uint_as_float(v & 0xFFFF0000u); }
typedef __bf16 nbf2 __attribute__((ext_vector_type(2)));
__device__ __forceinline__ unsigned pk2(float a, float b) { nbf2 t = {(__bf16)a, (__bf16)b}; return __builtin_bit_cast(unsigned, t); }
__device__ __forceinline__ float rbf(float a) { return bflo(pk2(a, 0.f)); }
__device__ __forceinline__ f32x4 mfma16(bf16x8 a, bf16x8 b, f32x4 c) { return __builtin_amdgcn_mfma_f32_16x16x32_bf16(a, b, c, 0, 0, 0); }
__device__ __forceinline__ bf16x4 trread(const char* lds_base, int off) { return __builtin_amdgcn_ds_read_tr16_b64_v4i16((LAS bf16x4*)(lds_base + off)); }
__device__ __forceinline__ bf16x8 cat8(bf16x4 a, bf16x4 b) { return __builtin_shufflevector(a, b, 0, 1, 2, 3, 4, 5, 6, 7); }
__device__ __forceinline__ v4u ld16c(__amdgpu_buffer_rsrc_t r, unsigned off) { return __builtin_amdgcn_raw_buffer_load_b128(r, off, 0, 16); }
__device__ __forceinline__ v2u ld8c(__amdgpu_buffer_rsrc_t r, unsigned off) { return __builtin_amdgcn_raw_buffer_load_b64(r, off, 0, 16); }
__device__ __forceinline__ void st16c(__amdgpu_buffer_rsrc_t r, unsigned off, v4u v) { __builtin_amdgcn_raw_buffer_store_b128(v, r, off, 0, 16); }
__device__ __forceinline__ bool okv(v4u v) { return v.x != SENT && v.y != SENT && v.z != SENT && v.w != SENT; }
__device__ __forceinline__ bf16x8 asf(v4u v) { return __builtin_bit_cast(bf16x8, v); }
#define SB() __builtin_amdgcn_sched_barrier(0)
__device__ __forceinline__ float xmax16(float x) { unsigned u = __float_as_uint(x); auto r = __builtin_amdgcn_permlane16_swap(u, u, false, false); return fmaxf(__uint_as_float(r[0]), __uint_as_float(r[1])); }
__device__ __forceinline__ float xmax32(float x) { unsigned u = __float_as_uint(x); auto r = __builtin_amdgcn_permlane32_swap(u, u, false, false); return fmaxf(__uint_as_float(r[0]), __uint_as_float(r[1])); }
__device__ __forceinline__ float xsum16(float x) { unsigned u = __float_as_uint(x); auto r = __builtin_amdgcn_permlane16_swap(u, u, false, false); return __uint_as_float(r[0]) + __uint_as_float(r[1]); }
__device__ __forceinline__ float xsum32(float x) { unsigned u = __float_as_uint(x); auto r = __builtin_amdgcn_permlane32_swap(u, u, false, false); return __uint_as_float(r[0]) + __uint_as_float(r[1]); }
__device__ __forceinline__ v4u pair16(f32x4 a, f32x4 b) {
    unsigned a0 = pk2(a[0], a[1]), a1 = pk2(a[2], a[3]), b0 = pk2(b[0], b[1]), b1 = pk2(b[2], b[3]);
    auto x0 = __builtin_amdgcn_permlane16_swap(a0, b0, false, false); auto x1 = __builtin_amdgcn_permlane16_swap(a1, b1, false, false);
    return (v4u){x0[0], x1[0], x0[1], x1[1]};
}
__device__ __forceinline__ void bar_lds() { asm volatile("s_waitcnt lgkmcnt(0)\n\ts_barrier" ::: "memory"); }
__device__ __forceinline__ void drain_vm() { asm volatile("s_waitcnt vmcnt(0)" ::: "memory"); }
__device__ __forceinline__ bool wait_cnt(gu32* ctl, int idx, unsigned target) {
    for (unsigned spins = 0;; ++spins) {
        asm volatile("" ::: "memory");
        unsigned v = __builtin_amdgcn_readfirstlane(__hip_atomic_load(ctl + idx, RLX_AGENT));
        if (v >= target) return false;
        if (spins > kSpinCap) { __hip_atomic_store(ctl + CW_TMO, 1u, RLX_AGENT); return true; }
        if ((spins & 63u) == 63u && __builtin_amdgcn_readfirstlane(__hip_atomic_load(ctl + CW_TMO, RLX_AGENT))) return true;
        __builtin_amdgcn_s_sleep(2);
    }
}

__device__ __forceinline__ bool spin_dead(unsigned& spins, gu32* ctl) {
    ++spins;
    if (spins > kSpinCap) { __hip_atomic_store(ctl + CW_TMO, 1u, RLX_AGENT); return true; }
    if ((spins & 1023u) == 1023u) return __builtin_amdgcn_readfirstlane(__hip_atomic_load(ctl + CW_TMO, RLX_AGENT)) != 0;
    return false;
}

__device__ __forceinline__ int boff192(int row, int ch) { int f = ((row >> 1) & 1) | (((row >> 3) & 1) << 1); return row * 384 + (((ch >> 1) ^ f) << 5) + ((ch & 1) << 4); }
template <int RB> __device__ __forceinline__ int boffP(int row, int ch) { int f = (row & 3) | (((row >> 3) & 1) << 2); return row * RB + (((ch >> 1) ^ f) << 5) + ((ch & 1) << 4); }
__device__ __forceinline__ bf16x8 trfrag192(const char* base, int r0, int c, int lane) {
    const int g = lane >> 4, q = (lane & 15) >> 2, p = lane & 3;
    const int f = ((q >> 1) & 1) | ((g & 1) << 1);
    const int a = (r0 + 8 * g + q) * 384 + ((c ^ f) << 5) + 8 * p;
    return cat8(trread(base, a), trread(base, a + 4 * 384));
}
template <int RB> __device__ __forceinline__ bf16x8 trfragP(const char* base, int r0, int c, int lane) {
    const int g = lane >> 4, q = (lane & 15) >> 2, p = lane & 3;
    const int f = q | ((g & 1) << 2);
    const int a = (r0 + 8 * g + q) * RB + ((c ^ f) << 5) + 8 * p;
    return cat8(trread(base, a), trread(base, a + 4 * RB));
}

#define SK0V 52
constexpr int SK0 = SK0V;
__host__ __device__ constexpr int slot_size(int t) {
    int n = 0;
    if (t + 2 < 64) n += 4;
    if (t == 0) n += 8;
    if (t >= 1 && t <= 64) n += 10;
    { int s = t - 2; if (s >= 0 && s < SK0) n += (s % 4 == 3) ? 16 : 0; else if (s >= SK0 && s < 64) n += 32; }
    { int s = t - 3; if (s >= 0 && s < SK0) n += (s % 4 == 3) ? 4 : 0; else if (s >= SK0 && s < 64) n += 10; }
    return n;
}
__host__ __device__ constexpr int slot_task(int t, int v) {
    if (t == 0) { if (v < 8) return (2 << 16) | ((v >> 2) << 8) | (v & 3); v -= 8; }
    if (t + 2 < 64) { if (v < 4) return (2 << 16) | ((t + 2) << 8) | v; v -= 4; }
    if (t >= 1 && t <= 64) { if (v < 10) return (3 << 16) | ((t - 1) << 8) | v; v -= 10; }
    { int s = t - 2; if (s >= 0 && s < SK0) { if (s % 4 == 3) { if (v < 16) return (6 << 16) | ((s >> 2) << 8) | v; v -= 16; } } else if (s >= SK0 && s < 64) { if (v < 32) return (4 << 16) | (s << 8) | v; v -= 32; } }
    { int s = t - 3; if (s >= 0 && s < SK0) { if (s % 4 == 3) return (7 << 16) | ((s >> 2) << 8) | v; } else return (5 << 16) | (s << 8) | v; }
    return -1;
}
__host__ __device__ constexpr int group_size(int r) {
    int n = 0; if (r < 16) n += 64; if (r - 1 >= 0 && r - 1 < 16) n += 32;
    for (int j = 0; j < 4; ++j) { int t = 4 * (r - 4) + j; if (t >= 0 && t < 67) n += slot_size(t); }
    return n;
}
__host__ __device__ constexpr int total_tasks() { int n = 0; for (int r = 0; r < 22; ++r) n += group_size(r); return n; }
constexpr int NTASK = total_tasks();
static_assert(NTASK <= 4096, "task table");
constexpr int NPRE = 64 + 64;
constexpr int NTASK_ALL = NTASK + NPRE;
struct TaskTab { int v[NTASK_ALL]; };
constexpr TaskTab make_tasks() {
    TaskTab T{}; int pos = 0;
    for (int i = 0; i < 4; ++i) T.v[pos++] = (8 << 16) | (i << 8);
    for (int i = 0; i < 64; ++i) T.v[pos++] = (9 << 16) | (i << 8);
    for (int r = 0; r < 22; ++r) {
        if (r + 1 < 16) { for (int i = 0; i < 4; ++i) T.v[pos++] = (8 << 16) | ((4 * (r + 1) + i) << 8); }
        if (r < 16) { for (int h = 0; h < 32; ++h) T.v[pos++] = (0 << 16) | (r << 8) | h; for (int h = 0; h < 32; ++h) T.v[pos++] = (10 << 16) | (r << 8) | h; }
        if (r - 1 >= 0 && r - 1 < 16) { for (int h = 0; h < 32; ++h) T.v[pos++] = (1 << 16) | ((r - 1) << 8) | h; }
        for (int j = 0; j < 4; ++j) { int t = 4 * (r - 4) + j; if (t >= 0 && t < 67) { int n = slot_size(t); for (int q = 0; q < n; ++q) T.v[pos++] = slot_task(t, q); } }
    }
    return T;
}
__device__ const TaskTab g_tasks = make_tasks();

__global__ void __launch_bounds__(256) k_pre(const u16* __restrict__ c, const u16* __restrict__ wmod, const u16* __restrict__ mem0, char* ws) {
    __shared__ float sc[640]; __shared__ float red[64][33];
    const int b = blockIdx.x, tid = threadIdx.x;
    if (b < 120) {
        for (int i = tid; i < 640; i += 256) { float v = bf2f(c[i]); sc[i] = rbf(v / (1.f + __expf(-v))); }
        __syncthreads();
        const int kg = tid >> 2, cq = tid & 3;
        v4u wv[10];
#pragma unroll
        for (int j = 0; j < 10; ++j) wv[j] = *(const v4u*)(wmod + (size_t)(kg * 10 + j) * 3840 + b * 32 + cq * 8);
        float a[8] = {0.f, 0.f, 0.f, 0.f, 0.f, 0.f, 0.f, 0.f};
#pragma unroll
        for (int j = 0; j < 10; ++j) { float s = sc[kg * 10 + j];
            a[0] += s * bflo(wv[j].x); a[1] += s * bfhi(wv[j].x); a[2] += s * bflo(wv[j].y); a[3] += s * bfhi(wv[j].y);
            a[4] += s * bflo(wv[j].z); a[5] += s * bfhi(wv[j].z); a[6] += s * bflo(wv[j].w); a[7] += s * bfhi(wv[j].w); }
#pragma unroll
        for (int j = 0; j < 8; ++j) red[kg][cq * 8 + j] = a[j];
        __syncthreads();
        if (tid < 32) { float s = 0.f; for (int k = 0; k < 64; ++k) s += red[k][tid]; ((float*)(ws + OFF_MOD))[b * 32 + tid] = rbf(s); }
    } else if (b == 120) {
        for (int ci = tid; ci < 640; ci += 256) { int i = ci & 7, kg = ci >> 3; *(v4u*)(ws + OFF_MEM + ci * 16) = *(const v4u*)(mem0 + i * 640 + kg * 8); }
        for (int i = tid; i < 8192; i += 256) ((unsigned*)(ws + OFF_CTL))[i] = 0;
    }
}

__global__ void __launch_bounds__(256) k_ln(const u16* __restrict__ x, const float* __restrict__ mod, int so, int co, u16* __restrict__ out) {
    const int row = blockIdx.x * 4 + (threadIdx.x >> 6), lane = threadIdx.x & 63;
    const unsigned* xr = (const unsigned*)(x + (size_t)row * D_);
    float v[10]; float s = 0.f;
#pragma unroll
    for (int j = 0; j < 5; ++j) { unsigned u = xr[j * 64 + lane]; v[2 * j] = bflo(u); v[2 * j + 1] = bfhi(u); s += v[2 * j] + v[2 * j + 1]; }
#pragma unroll
    for (int m = 32; m >= 1; m >>= 1) s += __shfl_xor(s, m);
    const float mean = s * (1.f / D_); float q = 0.f;
#pragma unroll
    for (int j = 0; j < 10; ++j) { float d = v[j] - mean; q += d * d; }
#pragma unroll
    for (int m = 32; m >= 1; m >>= 1) q += __shfl_xor(q, m);
    const float rstd = 1.f / sqrtf(q * (1.f / D_) + 1e-5f);
    unsigned* orow = (unsigned*)(out + (size_t)row * D_);
#pragma unroll
    for (int j = 0; j < 5; ++j) { int c0 = (j * 64 + lane) * 2;
        float a = (v[2 * j] - mean) * rstd * (1.f + mod[co + c0]) + mod[so + c0];
        float b = (v[2 * j + 1] - mean) * rstd * (1.f + mod[co + c0 + 1]) + mod[so + c0 + 1];
        orow[j * 64 + lane] = pk2(a, b); }
}

constexpr int GA_B = 128 * 128;
constexpr int GB_B = 64 * 384;
constexpr int GBUF = GA_B + GB_B;
#define PFD 2
#define PFD2 2
template <int NK, int PD, int AUXA, class AT>
__device__ __forceinline__ void gemm4(char* lds, __amdgpu_buffer_rsrc_t rsA, unsigned vA, unsigned sA, unsigned lda2,
                                      __amdgpu_buffer_rsrc_t rsB0, __amdgpu_buffer_rsrc_t rsB1, unsigned vB0, unsigned vB1, unsigned sB, unsigned ldb2, int cB0, int cB1,
                                      AT atr, f32x4 (&acc)[4][6]) {
    int tid = threadIdx.x; asm volatile("" : "+v"(tid));
    const int lane = tid & 63, w = tid >> 6, wm = w >> 1, wn = w & 1, g = lane >> 4, l15 = lane & 15;
#pragma unroll
    for (int a = 0; a < 4; ++a)
#pragma unroll
        for (int b = 0; b < 6; ++b) acc[a][b] = (f32x4){0.f, 0.f, 0.f, 0.f};
    v4u ra[PD][4], rb[PD][8];
    const int ar = tid >> 3, ach = tid & 7, br = (tid < 192 ? tid : 0) / 12;
    const int bf = ((br >> 1) & 1) | (((br >> 3) & 1) << 1);
    const int bo0 = GA_B + br * 384 + (((cB0 >> 1) ^ bf) << 5) + ((cB0 & 1) << 4);
    const int bo1 = GA_B + br * 384 + (((cB1 >> 1) ^ bf) << 5) + ((cB1 & 1) << 4);
    const int ao = ar * 128 + ((ach ^ (ar & 7)) << 4);
    auto gload = [&](int kc, v4u (&a4)[4], v4u (&b8)[8]) {
#pragma unroll
        for (int i = 0; i < 4; ++i) a4[i] = __builtin_amdgcn_raw_buffer_load_b128(rsA, vA, sA + kc * 128 + i * 32 * lda2, AUXA);
#pragma unroll
        for (int i = 0; i < 4; ++i) {
            b8[i] = __builtin_amdgcn_raw_buffer_load_b128(rsB0, vB0, sB + (kc * 64 + 16 * i) * ldb2, 0);
            b8[4 + i] = __builtin_amdgcn_raw_buffer_load_b128(rsB1, vB1, sB + (kc * 64 + 16 * i) * ldb2, 0);
        }
    };
    auto lstore = [&](int kc, char* buf, v4u (&a4)[4], v4u (&b8)[8]) {
#pragma unroll
        for (int i = 0; i < 4; ++i) *(v4u*)(buf + ao + i * 4096) = atr(kc, ar + 32 * i, ach, a4[i]);
        if (tid < 192) {
#pragma unroll
            for (int i = 0; i < 4; ++i) { *(v4u*)(buf + bo0 + i * 6144) = b8[i]; *(v4u*)(buf + bo1 + i * 6144) = b8[4 + i]; }
        }
    };
#pragma unroll
    for (int k = 0; k < PD; ++k) gload(k, ra[k], rb[k]);
    lstore(0, lds, ra[0], rb[0]);
    if (PD < NK) gload(PD, ra[0], rb[0]);
    __syncthreads();
#pragma unroll 1
    for (int k0 = 0; k0 < NK; k0 += PD) {
#pragma unroll
        for (int u = 0; u < PD; ++u) {
            const int kc = k0 + u;
            char* cur = lds + (kc & 1) * GBUF; char* nxt = lds + ((kc + 1) & 1) * GBUF;
#pragma unroll
            for (int ks = 0; ks < 2; ++ks) {
                bf16x8 af[4], bfr[6];
#pragma unroll
                for (int mt = 0; mt < 4; ++mt) { int r = wm * 64 + mt * 16 + l15; af[mt] = *(const bf16x8*)(cur + r * 128 + (((4 * ks + g) ^ (r & 7)) << 4)); }
#pragma unroll
                for (int nt = 0; nt < 6; ++nt) bfr[nt] = trfrag192(cur + GA_B, 32 * ks, wn * 6 + nt, lane);
#pragma unroll
                for (int mt = 0; mt < 4; ++mt)
#pragma unroll
                    for (int nt = 0; nt < 6; ++nt) acc[mt][nt] = mfma16(bfr[nt], af[mt], acc[mt][nt]);
            }
            if (kc + 1 < NK) {
                lstore(kc + 1, nxt, ra[(u + 1) % PD], rb[(u + 1) % PD]);
                if (kc + 1 + PD < NK) gload(kc + 1 + PD, ra[(u + 1) % PD], rb[(u + 1) % PD]);
            }
            __syncthreads();
        }
    }
}
__device__ __forceinline__ __amdgpu_buffer_rsrc_t mkrs(const void* p, unsigned bytes) { return __builtin_amdgcn_make_buffer_rsrc((void*)p, 0, (int)bytes, 0x00020000); }

constexpr int KA_VOFF = 2 * GBUF;
constexpr int KA_VSTR = 400;
__device__ __forceinline__ void taskA(char* lds, int rbk, int h, int qonly, const u16* __restrict__ Wq, const u16* __restrict__ Wk, const u16* __restrict__ Wv,
                                      const u16* __restrict__ Wo, char* ws, __amdgpu_buffer_rsrc_t rw) {
    int tid = threadIdx.x; asm volatile("" : "+v"(tid));
    const int lane = tid & 63, w = tid >> 6, wm = w >> 1, wn = w & 1, g = lane >> 4, l15 = lane & 15;
    f32x4 acc[4][6];
#pragma unroll 1
    for (int mat = qonly; mat <= qonly; ++mat) {
        const __amdgpu_buffer_rsrc_t rW = mkrs(mat == 0 ? Wk : (mat == 1 ? Wq : Wv), (unsigned)(D_ * HE * 2));
        const int bt = tid < 192 ? tid : 0, brr = bt / 12, bc = bt % 12;
        gemm4<10, PFD, 16>(lds, rw, (unsigned)(((tid >> 3) * D_ + (tid & 7) * 8) * 2), (unsigned)OFF_H + (unsigned)(rbk * 128 * D_ * 2), D_ * 2,
                          rW, rW, (unsigned)((brr * HE + bc * 8) * 2), (unsigned)((brr * HE + (12 + bc) * 8) * 2), (unsigned)(h * E_ * 2), HE * 2, bc, 12 + bc,
                          [](int, int, int, v4u v) { return v; }, acc);
        if (mat < 2) {
            const unsigned ob = (unsigned)(mat == 0 ? OFF_K : OFF_Q);
#pragma unroll
            for (int mt = 0; mt < 4; ++mt)
#pragma unroll
                for (int nt = 0; nt < 6; nt += 2) {
                    int tok = rbk * 128 + wm * 64 + mt * 16 + l15, e = wn * 96 + (nt + (g & 1)) * 16 + 8 * (g >> 1);
                    st16c(rw, ob + (unsigned)(tok * HE + h * E_ + e) * 2u, pair16(acc[mt][nt], acc[mt][nt + 1]));
                }
        } else {
#pragma unroll
            for (int mt = 0; mt < 4; ++mt)
#pragma unroll
                for (int nt = 0; nt < 6; ++nt) {
                    int tok = wm * 64 + mt * 16 + l15, e = wn * 96 + nt * 16 + 4 * g;
                    *(v2u*)(lds + KA_VOFF + tok * KA_VSTR + e * 2) = (v2u){pk2(acc[mt][nt][0], acc[mt][nt][1]), pk2(acc[mt][nt][2], acc[mt][nt][3])};
                }
        }
    }
    if (qonly != 2) return;
    __syncthreads();
    bf16x8 vf[4][6];
#pragma unroll
    for (int mt = 0; mt < 4; ++mt)
#pragma unroll
        for (int t = 0; t < 6; ++t) vf[mt][t] = *(const bf16x8*)(lds + KA_VOFF + (wm * 64 + mt * 16 + l15) * KA_VSTR + (4 * t + g) * 16);
    const __amdgpu_buffer_rsrc_t rWo = mkrs(Wo, (unsigned)(HE * D_ * 2));
    const int r0 = tid >> 4, c0 = tid & 15;
    const int wof = r0 * 256 + (((c0 >> 1) ^ ((r0 & 3) | (((r0 >> 3) & 1) << 2))) << 5) + ((c0 & 1) << 4);
    v4u rb[12];
    auto gl = [&](int nc) {
#pragma unroll
        for (int i = 0; i < 12; ++i) rb[i] = __builtin_amdgcn_raw_buffer_load_b128(rWo, (unsigned)((r0 * D_ + c0 * 8) * 2), (unsigned)(((h * E_ + 16 * i) * D_ + nc * 128) * 2), 0);
    };
    gl(0);
#pragma unroll 1
    for (int nc = 0; nc < 5; ++nc) {
        __syncthreads();
#pragma unroll
        for (int i = 0; i < 12; ++i) *(v4u*)(lds + wof + i * 4096) = rb[i];
        __syncthreads();
        if (nc + 1 < 5) gl(nc + 1);
        f32x4 ua[4][4];
#pragma unroll
        for (int mt = 0; mt < 4; ++mt)
#pragma unroll
            for (int nt = 0; nt < 4; ++nt) ua[mt][nt] = (f32x4){0.f, 0.f, 0.f, 0.f};
#pragma unroll
        for (int t = 0; t < 6; ++t) {
            bf16x8 wf[4];
#pragma unroll
            for (int nt = 0; nt < 4; ++nt) wf[nt] = trfragP<256>(lds, 32 * t, wn * 4 + nt, lane);
#pragma unroll
            for (int mt = 0; mt < 4; ++mt)
#pragma unroll
                for (int nt = 0; nt < 4; ++nt) ua[mt][nt] = mfma16(vf[mt][t], wf[nt], ua[mt][nt]);
        }
#pragma unroll
        for (int mt = 0; mt < 4; ++mt)
#pragma unroll
            for (int nt = 0; nt < 4; nt += 2) {
                int seg = rbk * 4 + wm * 2 + (mt >> 1), r = nc * 8 + wn * 4 + nt + (g & 1), jg = 2 * (mt & 1) + (g >> 1);
                unsigned off = (unsigned)OFF_UX + (unsigned)(((((seg * 32 + h) * 40 + r) * 4 + jg) * 16 + l15) * 16);
                st16c(rw, off, pair16(ua[mt][nt], ua[mt][nt + 1]));
            }
    }
}

__device__ __forceinline__ bool taskP(int s, int hq, char* ws, __amdgpu_buffer_rsrc_t rw) {
    int tid = threadIdx.x; asm volatile("" : "+v"(tid));
    const int lane = tid & 63, w = tid >> 6, g = lane >> 4, l15 = lane & 15;
    const float scale = 0.07216878364870322f;
    bool dead = false;
    v4u qf[2][6][2], kf[2][6][2], km[2][6];
#pragma unroll
    for (int hh = 0; hh < 2; ++hh) {
        const int h = hq * 8 + w * 2 + hh;
#pragma unroll
        for (int t = 0; t < 6; ++t)
#pragma unroll
            for (int tt = 0; tt < 2; ++tt) {
                unsigned o = (unsigned)(((s * 32 + 16 * tt + l15) * HE + h * E_ + 32 * t + 8 * g) * 2);
                qf[hh][t][tt] = ld16c(rw, (unsigned)OFF_Q + o); kf[hh][t][tt] = ld16c(rw, (unsigned)OFF_K + o);
            }
    }
    {
        unsigned spins = 0;
        const unsigned kb = (unsigned)OFF_KM + (unsigned)((((s * 32 + hq * 8 + w * 2) * 8 + (lane & 7)) * E_ + 8 * g) * 2);
        while (true) {
            asm volatile("" ::: "memory");
            bool ok = true;
#pragma unroll
            for (int hh = 0; hh < 2; ++hh)
#pragma unroll
                for (int t = 0; t < 6; ++t) km[hh][t] = ld16c(rw, kb + hh * (8 * E_ * 2) + t * 64);
#pragma unroll
            for (int hh = 0; hh < 2; ++hh)
#pragma unroll
                for (int t = 0; t < 6; ++t) ok &= okv(km[hh][t]);
            if (__all(ok)) break;
            if (++spins > kSpinCap) { dead = true; break; }
            __builtin_amdgcn_s_sleep(4);
        }
    }
#pragma unroll
    for (int hh = 0; hh < 2; ++hh) {
        const int h = hq * 8 + w * 2 + hh;
        f32x4 sc[3][2];
#pragma unroll
        for (int a = 0; a < 3; ++a) for (int b = 0; b < 2; ++b) sc[a][b] = (f32x4){0.f, 0.f, 0.f, 0.f};
#pragma unroll
        for (int t = 0; t < 6; ++t)
#pragma unroll
            for (int tt = 0; tt < 2; ++tt) {
                sc[0][tt] = mfma16(asf(km[hh][t]), asf(qf[hh][t][tt]), sc[0][tt]);
                sc[1][tt] = mfma16(asf(kf[hh][t][0]), asf(qf[hh][t][tt]), sc[1][tt]);
                sc[2][tt] = mfma16(asf(kf[hh][t][1]), asf(qf[hh][t][tt]), sc[2][tt]);
            }
#pragma unroll
        for (int tt = 0; tt < 2; ++tt) {
            const int tok = 16 * tt + l15; float mx = -3.0e38f;
#pragma unroll
            for (int a = 0; a < 3; ++a)
#pragma unroll
                for (int r = 0; r < 4; ++r) {
                    bool valid = (a == 0) ? (g < 2) : (16 * (a - 1) + 4 * g + r <= tok);
                    float v = valid ? rbf(rbf(sc[a][tt][r]) * scale) : -3.0e38f; sc[a][tt][r] = v; mx = fmaxf(mx, v);
                }
            mx = xmax32(xmax16(mx));
            float sum = 0.f;
#pragma unroll
            for (int a = 0; a < 3; ++a)
#pragma unroll
                for (int r = 0; r < 4; ++r) { float v = __expf(sc[a][tt][r] - mx); sc[a][tt][r] = v; sum += v; }
            sum = xsum32(xsum16(sum));
            const float inv = 1.f / sum;
            const unsigned pr = (unsigned)OFF_PT + (unsigned)(((s * 32 + tok) * 1280 + h * 40) * 2);
            v4u x = pair16(sc[1][tt] * inv, sc[2][tt] * inv), y = pair16(sc[0][tt] * inv, sc[0][tt] * inv);
            st16c(rw, pr + (1 + (g >> 1) + 2 * (g & 1)) * 16, x);
            if (g == 0) st16c(rw, pr, y);
        }
    }
    return dead;
}

constexpr int O_PITCH = 2576;
__device__ __forceinline__ bool taskO(char* lds, int s, int cq, const u16* __restrict__ x, char* ws, __amdgpu_buffer_rsrc_t rw, gu32* ctl) {
    int tid = threadIdx.x; asm volatile("" : "+v"(tid));
    const int lane = tid & 63, w = tid >> 6, g = lane >> 4, l15 = lane & 15;
    const int r0 = cq * 4 + w;
    bool dead = false;
    wait_cnt(ctl, CW_A + 32 * (s >> 2), 68);
    v4u ua[40];
    auto ldu = [&]() {
#pragma unroll
        for (int T = 0; T < 40; ++T) {
            int cg = 4 * T + g, h = cg / 5, jg = cg % 5;
            unsigned uo = (jg == 0) ? (unsigned)OFF_UM + (unsigned)(((s * 32 + h) * 40 + r0) * 256 + l15 * 16)
                                    : (unsigned)OFF_UX + (unsigned)(((((s * 32 + h) * 40 + r0) * 4 + jg - 1) * 16 + l15) * 16);
            ua[T] = ld16c(rw, uo);
        }
    };
    ldu();
    wait_cnt(ctl, CW_P + 32 * s, 4);
    {
        const int row = tid >> 3, c0 = tid & 7;
        v4u pa[20];
#pragma unroll
        for (int j = 0; j < 20; ++j) pa[j] = ld16c(rw, (unsigned)OFF_PT + (unsigned)(((s * 32 + row) * 1280 + (c0 + 8 * j) * 8) * 2));
#pragma unroll
        for (int j = 0; j < 20; ++j) *(v4u*)(lds + row * O_PITCH + (c0 + 8 * j) * 16) = pa[j];
    }
    {
        unsigned spins = 0;
        while (true) {
            bool ok = true;
#pragma unroll
            for (int T = 0; T < 40; ++T) { bool um = ((4 * T + g) % 5 == 0); ok &= okv(ua[T]) || !um; }
            if (__all(ok)) break;
            if (spin_dead(spins, ctl)) { dead = true; break; }
            __builtin_amdgcn_s_sleep(4);
            asm volatile("" ::: "memory");
            ldu();
        }
    }
    __syncthreads();
    f32x4 acc[2][2] = {{{0.f, 0.f, 0.f, 0.f}, {0.f, 0.f, 0.f, 0.f}}, {{0.f, 0.f, 0.f, 0.f}, {0.f, 0.f, 0.f, 0.f}}};
#pragma unroll
    for (int T = 0; T < 40; ++T)
#pragma unroll
        for (int tt = 0; tt < 2; ++tt) {
            bf16x8 pb = *(const bf16x8*)(lds + (16 * tt + l15) * O_PITCH + (4 * T + g) * 16);
            acc[tt][T & 1] = mfma16(asf(ua[T]), pb, acc[tt][T & 1]);
        }
    const float* mod = (const float*)(ws + OFF_MOD);
#pragma unroll
    for (int tt = 0; tt < 2; ++tt) {
        int tok = s * 32 + 16 * tt + l15, c = 16 * r0 + 4 * g;
        v2u xv = *(const v2u*)(x + (size_t)tok * D_ + c);
        float xs[4] = {bflo(xv.x), bfhi(xv.x), bflo(xv.y), bfhi(xv.y)}; f32x4 o; f32x4 av = acc[tt][0] + acc[tt][1];
#pragma unroll
        for (int j = 0; j < 4; ++j) o[j] = xs[j] + rbf(mod[2 * 640 + c + j] * rbf(av[j]));
        v4u pv = pair16(o, o);
        if ((g & 1) == 0) st16c(rw, (unsigned)OFF_X1 + (unsigned)((tok * D_ + 16 * r0 + 8 * (g >> 1)) * 2), pv);
    }
    return dead;
}

constexpr int F1_APITCH = 1296;
constexpr int F1_BOFF = 32 * F1_APITCH;
constexpr int F1_ROFF = F1_BOFF;
__device__ __forceinline__ void taskF1(char* lds, int s, int ct, const u16* __restrict__ w1, const u16* __restrict__ w3, char* ws, __amdgpu_buffer_rsrc_t rw, gu32* ctl) {
    int tid = threadIdx.x; asm volatile("" : "+v"(tid));
    const int lane = tid & 63, w = tid >> 6, g = lane >> 4, l15 = lane & 15;
    const __amdgpu_buffer_rsrc_t r1 = mkrs(w1, D_ * F_ * 2), r3 = mkrs(w3, D_ * F_ * 2);
    v4u bw[5][6];
    unsigned vo[3]; int lo[3];
#pragma unroll
    for (int i = 0; i < 3; ++i) { int idx = lane + 64 * i, row = idx / 6, c = idx % 6; vo[i] = (unsigned)((row * F_ + c * 8) * 2);
        lo[i] = row * 192 + c * 16; }
#pragma unroll
    for (int j = 0; j < 5; ++j)
#pragma unroll
        for (int i = 0; i < 3; ++i) {
            unsigned so = (unsigned)(((5 * w + j) * 32 * F_ + ct * 48) * 2);
            bw[j][i] = __builtin_amdgcn_raw_buffer_load_b128(r1, vo[i], so, 0); bw[j][3 + i] = __builtin_amdgcn_raw_buffer_load_b128(r3, vo[i], so, 0);
        }
    wait_cnt(ctl, CW_X + 32 * s, 10);
    {
        const int row = tid >> 3, c0 = tid & 7;
        v4u xa[10];
#pragma unroll
        for (int j = 0; j < 10; ++j) xa[j] = ld16c(rw, (unsigned)OFF_X1 + (unsigned)(((s * 32 + row) * D_ + (c0 + 8 * j) * 8) * 2));
        float sum = 0.f;
#pragma unroll
        for (int j = 0; j < 10; ++j) sum += bflo(xa[j].x) + bfhi(xa[j].x) + bflo(xa[j].y) + bfhi(xa[j].y) + bflo(xa[j].z) + bfhi(xa[j].z) + bflo(xa[j].w) + bfhi(xa[j].w);
        sum += __shfl_xor(sum, 1); sum += __shfl_xor(sum, 2); sum += __shfl_xor(sum, 4);
        const float mean = sum * (1.f / D_); float q = 0.f;
#pragma unroll
        for (int j = 0; j < 10; ++j) { float d;
            d = bflo(xa[j].x) - mean; q += d * d; d = bfhi(xa[j].x) - mean; q += d * d; d = bflo(xa[j].y) - mean; q += d * d; d = bfhi(xa[j].y) - mean; q += d * d;
            d = bflo(xa[j].z) - mean; q += d * d; d = bfhi(xa[j].z) - mean; q += d * d; d = bflo(xa[j].w) - mean; q += d * d; d = bfhi(xa[j].w) - mean; q += d * d; }
        q += __shfl_xor(q, 1); q += __shfl_xor(q, 2); q += __shfl_xor(q, 4);
        const float rstd = 1.f / sqrtf(q * (1.f / D_) + 1e-5f);
        const float* mod = (const float*)(ws + OFF_MOD);
#pragma unroll
        for (int j = 0; j < 10; ++j) {
            const int c = (c0 + 8 * j) * 8;
            f32x4 m0 = *(const f32x4*)(mod + 4 * 640 + c), m1 = *(const f32x4*)(mod + 4 * 640 + c + 4), s0 = *(const f32x4*)(mod + 3 * 640 + c), s1 = *(const f32x4*)(mod + 3 * 640 + c + 4);
            v4u u = xa[j], o;
            o.x = pk2((bflo(u.x) - mean) * rstd * (1.f + m0[0]) + s0[0], (bfhi(u.x) - mean) * rstd * (1.f + m0[1]) + s0[1]);
            o.y = pk2((bflo(u.y) - mean) * rstd * (1.f + m0[2]) + s0[2], (bfhi(u.y) - mean) * rstd * (1.f + m0[3]) + s0[3]);
            o.z = pk2((bflo(u.z) - mean) * rstd * (1.f + m1[0]) + s1[0], (bfhi(u.z) - mean) * rstd * (1.f + m1[1]) + s1[1]);
            o.w = pk2((bflo(u.w) - mean) * rstd * (1.f + m1[2]) + s1[2], (bfhi(u.w) - mean) * rstd * (1.f + m1[3]) + s1[3]);
            *(v4u*)(lds + row * F1_APITCH + (c0 + 8 * j) * 16) = o;
        }
    }
    __syncthreads();
    f32x4 acc[2][6];
#pragma unroll
    for (int a = 0; a < 2; ++a) for (int b = 0; b < 6; ++b) acc[a][b] = (f32x4){0.f, 0.f, 0.f, 0.f};
    char* bb = lds + F1_BOFF + w * 12288;
    int so2[6];
#pragma unroll
    for (int i = 0; i < 3; ++i) { int idx = lane + 64 * i, row = idx / 6, c = idx % 6, f = (row >> 3) & 1;
        so2[i] = row * 192 + (((c >> 1) ^ f) << 5) + ((c & 1) << 4); so2[3 + i] = row * 192 + ((((6 + c) >> 1) ^ f) << 5) + ((c & 1) << 4); }
    const int q4 = (lane & 15) >> 2, p4 = lane & 3;
#pragma unroll
    for (int j = 0; j < 5; ++j) {
        char* buf = bb + (j & 1) * 6144;
#pragma unroll
        for (int i = 0; i < 6; ++i) *(v4u*)(buf + so2[i]) = bw[j][i];
        bf16x8 af[2], bfr[6];
#pragma unroll
        for (int mt = 0; mt < 2; ++mt) af[mt] = *(const bf16x8*)(lds + (16 * mt + l15) * F1_APITCH + (4 * (5 * w + j) + g) * 16);
#pragma unroll
        for (int nt = 0; nt < 6; ++nt) { const int a = (8 * g + q4) * 192 + ((nt ^ (g & 1)) << 5) + 8 * p4; bfr[nt] = cat8(trread(buf, a), trread(buf, a + 4 * 192)); }
#pragma unroll
        for (int mt = 0; mt < 2; ++mt)
#pragma unroll
            for (int nt = 0; nt < 6; ++nt) acc[mt][nt] = mfma16(bfr[nt], af[mt], acc[mt][nt]);
    }
    __syncthreads();
    f32x4* red = (f32x4*)(lds + F1_ROFF);
#pragma unroll
    for (int mt = 0; mt < 2; ++mt)
#pragma unroll
        for (int nt = 0; nt < 6; ++nt) red[(w * 12 + mt * 6 + nt) * 64 + lane] = acc[mt][nt];
    __syncthreads();
    {
        const int mt = w & 1; f32x4 o[2];
#pragma unroll
        for (int n = 0; n < 2; ++n) {
            const int nt = (w < 2) ? n : 2;
            f32x4 ga = (red[(0 * 12 + mt * 6 + nt) * 64 + lane] + red[(1 * 12 + mt * 6 + nt) * 64 + lane]) + (red[(2 * 12 + mt * 6 + nt) * 64 + lane] + red[(3 * 12 + mt * 6 + nt) * 64 + lane]);
            f32x4 up = (red[(0 * 12 + mt * 6 + 3 + nt) * 64 + lane] + red[(1 * 12 + mt * 6 + 3 + nt) * 64 + lane]) + (red[(2 * 12 + mt * 6 + 3 + nt) * 64 + lane] + red[(3 * 12 + mt * 6 + 3 + nt) * 64 + lane]);
#pragma unroll
            for (int j = 0; j < 4; ++j) { float a = rbf(ga[j]), u = rbf(up[j]); float sl = rbf(a / (1.f + __expf(-a))); o[n][j] = sl * u; }
        }
        const int tok = s * 32 + 16 * mt + l15;
        v4u v = pair16(o[0], o[1]);
        if (w < 2) st16c(rw, (unsigned)OFF_ACT + (unsigned)((tok * F_ + ct * 48 + (g & 1) * 16 + 8 * (g >> 1)) * 2), v);
        else if ((g & 1) == 0) st16c(rw, (unsigned)OFF_ACT + (unsigned)((tok * F_ + ct * 48 + 32 + 8 * (g >> 1)) * 2), v);
    }
}

constexpr int F2_APITCH = 3088;
constexpr int F2_BOFF = 32 * F2_APITCH;
__device__ __forceinline__ void taskF2(char* lds, int s, int ct, const u16* __restrict__ w2, char* ws, u16* __restrict__ out, __amdgpu_buffer_rsrc_t rw, gu32* ctl) {
    int tid = threadIdx.x; asm volatile("" : "+v"(tid));
    const int lane = tid & 63, w = tid >> 6, g = lane >> 4, l15 = lane & 15;
    const __amdgpu_buffer_rsrc_t r2 = mkrs(w2, F_ * D_ * 2);
    v4u bw[12][4];
#pragma unroll
    for (int j = 0; j < 12; ++j)
#pragma unroll
        for (int i = 0; i < 4; ++i)
            bw[j][i] = __builtin_amdgcn_raw_buffer_load_b128(r2, (unsigned)((((lane >> 3) + 8 * i) * D_ + (lane & 7) * 8) * 2), (unsigned)(((12 * w + j) * 32 * D_ + ct * 64) * 2), 0);
    wait_cnt(ctl, CW_F + 32 * s, 32);
    {
        const int row = tid >> 3, c0 = tid & 7;
#pragma unroll
        for (int j0 = 0; j0 < 24; j0 += 12) {
            v4u xa[12];
#pragma unroll
            for (int j = 0; j < 12; ++j) xa[j] = ld16c(rw, (unsigned)OFF_ACT + (unsigned)(((s * 32 + row) * F_ + (c0 + 8 * (j0 + j)) * 8) * 2));
#pragma unroll
            for (int j = 0; j < 12; ++j) *(v4u*)(lds + row * F2_APITCH + (c0 + 8 * (j0 + j)) * 16) = xa[j];
        }
    }
    __syncthreads();
    f32x4 acc[2][4];
#pragma unroll
    for (int a = 0; a < 2; ++a) for (int b = 0; b < 4; ++b) acc[a][b] = (f32x4){0.f, 0.f, 0.f, 0.f};
    char* bb = lds + F2_BOFF + w * 8192;
    int so2[4];
#pragma unroll
    for (int i = 0; i < 4; ++i) { int row = (lane >> 3) + 8 * i, c = lane & 7, f = ((row >> 1) & 1) | (((row >> 3) & 1) << 1); so2[i] = row * 128 + (((c >> 1) ^ f) << 5) + ((c & 1) << 4); }
    const int q4 = (lane & 15) >> 2, p4 = lane & 3, ff = ((q4 >> 1) & 1) | ((g & 1) << 1);
#pragma unroll
    for (int j = 0; j < 12; ++j) {
        char* buf = bb + (j & 1) * 4096;
#pragma unroll
        for (int i = 0; i < 4; ++i) *(v4u*)(buf + so2[i]) = bw[j][i];
        bf16x8 af[2], bfr[4];
#pragma unroll
        for (int mt = 0; mt < 2; ++mt) af[mt] = *(const bf16x8*)(lds + (16 * mt + l15) * F2_APITCH + (4 * (12 * w + j) + g) * 16);
#pragma unroll
        for (int nt = 0; nt < 4; ++nt) { const int a = (8 * g + q4) * 128 + ((nt ^ ff) << 5) + 8 * p4; bfr[nt] = cat8(trread(buf, a), trread(buf, a + 4 * 128)); }
#pragma unroll
        for (int mt = 0; mt < 2; ++mt)
#pragma unroll
            for (int nt = 0; nt < 4; ++nt) acc[mt][nt] = mfma16(bfr[nt], af[mt], acc[mt][nt]);
    }
    __syncthreads();
    f32x4* red = (f32x4*)lds;
#pragma unroll
    for (int mt = 0; mt < 2; ++mt)
#pragma unroll
        for (int nt = 0; nt < 4; ++nt) red[(w * 8 + mt * 4 + nt) * 64 + lane] = acc[mt][nt];
    __syncthreads();
    {
        const float* mod = (const float*)(ws + OFF_MOD);
        const int mt = w & 1, n0 = (w >> 1) * 2; f32x4 o[2];
        const int tok = s * 32 + 16 * mt + l15;
#pragma unroll
        for (int n = 0; n < 2; ++n) {
            const int T = mt * 4 + n0 + n;
            f32x4 v = (red[(0 * 8 + T) * 64 + lane] + red[(1 * 8 + T) * 64 + lane]) + (red[(2 * 8 + T) * 64 + lane] + red[(3 * 8 + T) * 64 + lane]);
            const int c = ct * 64 + (n0 + n) * 16 + 4 * g;
            v2u xv = ld8c(rw, (unsigned)OFF_X1 + (unsigned)((tok * D_ + c) * 2));
            float xs[4] = {bflo(xv.x), bfhi(xv.x), bflo(xv.y), bfhi(xv.y)};
#pragma unroll
            for (int j = 0; j < 4; ++j) o[n][j] = xs[j] + rbf(mod[5 * 640 + c + j] * rbf(v[j]));
        }
        *(v4u*)(out + (size_t)tok * D_ + ct * 64 + (n0 + (g & 1)) * 16 + 8 * (g >> 1)) = pair16(o[0], o[1]);
    }
}

constexpr int B2_ST = 2 * GBUF;
__device__ __forceinline__ void taskF1B(char* lds, int rbk, int ct, const u16* __restrict__ w1, const u16* __restrict__ w3, char* ws, __amdgpu_buffer_rsrc_t rw) {
    int tid = threadIdx.x; asm volatile("" : "+v"(tid));
    const int lane = tid & 63, w = tid >> 6, wm = w >> 1, wn = w & 1, g = lane >> 4, l15 = lane & 15;
    float2* stats = (float2*)(lds + B2_ST);
    { const float* mod = (const float*)(ws + OFF_MOD); float* ml = (float*)(lds + B2_ST + 1024);
      for (int i = tid; i < 640; i += 256) { ml[i] = 1.f + mod[4 * 640 + i]; ml[640 + i] = mod[3 * 640 + i]; } }
    const unsigned xb = (unsigned)OFF_X1 + (unsigned)(rbk * 128 * D_ * 2);
#pragma unroll 1
    for (int rr = 0; rr < 4; ++rr) {
        const int row = rr * 32 + (tid >> 3), c0 = tid & 7;
        v4u xa[10];
#pragma unroll
        for (int j = 0; j < 10; ++j) xa[j] = ld16c(rw, xb + (unsigned)((row * D_ + (c0 + 8 * j) * 8) * 2));
        float sum = 0.f;
#pragma unroll
        for (int j = 0; j < 10; ++j) sum += bflo(xa[j].x) + bfhi(xa[j].x) + bflo(xa[j].y) + bfhi(xa[j].y) + bflo(xa[j].z) + bfhi(xa[j].z) + bflo(xa[j].w) + bfhi(xa[j].w);
        sum += __shfl_xor(sum, 1); sum += __shfl_xor(sum, 2); sum += __shfl_xor(sum, 4);
        const float mean = sum * (1.f / D_); float q = 0.f;
#pragma unroll
        for (int j = 0; j < 10; ++j) { float d;
            d = bflo(xa[j].x) - mean; q += d * d; d = bfhi(xa[j].x) - mean; q += d * d; d = bflo(xa[j].y) - mean; q += d * d; d = bfhi(xa[j].y) - mean; q += d * d;
            d = bflo(xa[j].z) - mean; q += d * d; d = bfhi(xa[j].z) - mean; q += d * d; d = bflo(xa[j].w) - mean; q += d * d; d = bfhi(xa[j].w) - mean; q += d * d; }
        q += __shfl_xor(q, 1); q += __shfl_xor(q, 2); q += __shfl_xor(q, 4);
        if (c0 == 0) stats[row] = make_float2(mean, 1.f / sqrtf(q * (1.f / D_) + 1e-5f));
    }
    __syncthreads();
    const float* modl = (const float*)(lds + B2_ST + 1024);
    f32x4 acc[4][6];
    const int bt = tid < 192 ? tid : 0, brr = bt / 12, bc = bt % 12;
    const unsigned vb = (unsigned)((brr * F_ + (bc / 6) * 48 + (bc % 6) * 8) * 2);
    gemm4<10, PFD, 16>(lds, rw, (unsigned)(((tid >> 3) * D_ + (tid & 7) * 8) * 2), xb, D_ * 2,
          mkrs(w1, D_ * F_ * 2), mkrs(w3, D_ * F_ * 2), vb, vb, (unsigned)(ct * 96 * 2), F_ * 2, (bc / 6) * 12 + bc % 6, (bc / 6) * 12 + 6 + bc % 6,
          [&](int kc, int r, int ch, v4u u) {
              float2 st = stats[r]; const int c = kc * 64 + ch * 8;
              f32x4 m0 = *(const f32x4*)(modl + c), m1 = *(const f32x4*)(modl + c + 4), s0 = *(const f32x4*)(modl + 640 + c), s1 = *(const f32x4*)(modl + 640 + c + 4);
              v4u o;
              o.x = pk2((bflo(u.x) - st.x) * st.y * m0[0] + s0[0], (bfhi(u.x) - st.x) * st.y * m0[1] + s0[1]);
              o.y = pk2((bflo(u.y) - st.x) * st.y * m0[2] + s0[2], (bfhi(u.y) - st.x) * st.y * m0[3] + s0[3]);
              o.z = pk2((bflo(u.z) - st.x) * st.y * m1[0] + s1[0], (bfhi(u.z) - st.x) * st.y * m1[1] + s1[1]);
              o.w = pk2((bflo(u.w) - st.x) * st.y * m1[2] + s1[2], (bfhi(u.w) - st.x) * st.y * m1[3] + s1[3]);
              return o; }, acc);
#pragma unroll
    for (int mt = 0; mt < 4; ++mt) {
        f32x4 o[4];
#pragma unroll
        for (int nt = 0; nt < 3; ++nt)
#pragma unroll
            for (int j = 0; j < 4; ++j) { float ga = rbf(acc[mt][nt][j]), up = rbf(acc[mt][nt + 3][j]); float sl = rbf(ga / (1.f + __expf(-ga))); o[nt][j] = sl * up; }
        int tok = rbk * 128 + wm * 64 + mt * 16 + l15;
        int f0 = ct * 96 + wn * 48 + (g & 1) * 16 + 8 * (g >> 1);
        st16c(rw, (unsigned)OFF_ACT + (unsigned)((tok * F_ + f0) * 2), pair16(o[0], o[1]));
        v4u y = pair16(o[2], o[2]);
        int f1 = ct * 96 + wn * 48 + 32 + 8 * (g >> 1);
        if ((g & 1) == 0) st16c(rw, (unsigned)OFF_ACT + (unsigned)((tok * F_ + f1) * 2), y);
    }
}
__device__ __forceinline__ void taskF2B(char* lds, int rbk, int ct, const u16* __restrict__ w2, char* ws, u16* __restrict__ out, __amdgpu_buffer_rsrc_t rw) {
    int tid = threadIdx.x; asm volatile("" : "+v"(tid));
    const int lane = tid & 63, w = tid >> 6, wm = w >> 1, wn = w & 1, g = lane >> 4, l15 = lane & 15;
    f32x4 acc[4][6];
    const unsigned ab = (unsigned)OFF_ACT + (unsigned)(rbk * 128 * F_ * 2);
    const int bt = tid < 192 ? tid : 0, brr = bt / 12, bc = bt % 12;
    const __amdgpu_buffer_rsrc_t rW = mkrs(w2, F_ * D_ * 2);
    int c0 = ct * 192 + bc * 8, c1 = ct * 192 + (12 + bc) * 8; if (c0 >= D_) c0 = 0; if (c1 >= D_) c1 = 0;
    gemm4<24, PFD2, 16>(lds, rw, (unsigned)(((tid >> 3) * F_ + (tid & 7) * 8) * 2), ab, F_ * 2,
          rW, rW, (unsigned)((brr * D_ + c0) * 2), (unsigned)((brr * D_ + c1) * 2), 0u, D_ * 2, bc, 12 + bc, [](int, int, int, v4u v) { return v; }, acc);
    const float* mod = (const float*)(ws + OFF_MOD);
#pragma unroll
    for (int mt = 0; mt < 4; ++mt)
#pragma unroll
        for (int nt = 0; nt < 6; ++nt) {
            int tok = rbk * 128 + wm * 64 + mt * 16 + l15, c = ct * 192 + wn * 96 + nt * 16 + 4 * g;
            if (c < D_) {
                v2u xv = ld8c(rw, (unsigned)OFF_X1 + (unsigned)((tok * D_ + c) * 2));
                float xs[4] = {bflo(xv.x), bfhi(xv.x), bflo(xv.y), bfhi(xv.y)}; float o[4];
#pragma unroll
                for (int j = 0; j < 4; ++j) o[j] = xs[j] + rbf(mod[5 * 640 + c + j] * rbf(acc[mt][nt][j]));
                *(v2u*)(out + (size_t)tok * D_ + c) = (v2u){pk2(o[0], o[1]), pk2(o[2], o[3])};
            }
        }
}

#define REDMODE 0
__device__ __forceinline__ void taskFill(int s, __amdgpu_buffer_rsrc_t rw) {
    int tid = threadIdx.x; asm volatile("" : "+v"(tid));
    const v4u sv = {SENT, SENT, SENT, SENT};
    for (int i = tid; i < 640; i += 256) st16c(rw, (unsigned)OFF_MEM + (unsigned)((s + 1) * 10240 + i * 16), sv);
    for (int i = tid; i < 1280; i += 256) st16c(rw, (unsigned)OFF_P + (unsigned)(s * 20480 + i * 16), sv);
    for (int i = tid; i < 20480; i += 256) st16c(rw, (unsigned)OFF_UM + (unsigned)(s * 327680 + i * 16), sv);
    for (int i = tid; i < 6144; i += 256) st16c(rw, (unsigned)OFF_KM + (unsigned)(s * 98304 + i * 16), sv);
}
__device__ __forceinline__ void taskLN(int sg, const u16* __restrict__ x, char* ws, __amdgpu_buffer_rsrc_t rw) {
    int tid = threadIdx.x; asm volatile("" : "+v"(tid));
    const float* mod = (const float*)(ws + OFF_MOD);
    {
        const int row = sg * 32 + (tid >> 3), c0 = tid & 7;
        v4u xa[10];
#pragma unroll
        for (int j = 0; j < 10; ++j) xa[j] = *(const v4u*)(x + (size_t)row * D_ + (c0 + 8 * j) * 8);
        float sum = 0.f;
#pragma unroll
        for (int j = 0; j < 10; ++j) sum += bflo(xa[j].x) + bfhi(xa[j].x) + bflo(xa[j].y) + bfhi(xa[j].y) + bflo(xa[j].z) + bfhi(xa[j].z) + bflo(xa[j].w) + bfhi(xa[j].w);
        sum += __shfl_xor(sum, 1); sum += __shfl_xor(sum, 2); sum += __shfl_xor(sum, 4);
        const float mean = sum * (1.f / D_); float q = 0.f;
#pragma unroll
        for (int j = 0; j < 10; ++j) { float d;
            d = bflo(xa[j].x) - mean; q += d * d; d = bfhi(xa[j].x) - mean; q += d * d; d = bflo(xa[j].y) - mean; q += d * d; d = bfhi(xa[j].y) - mean; q += d * d;
            d = bflo(xa[j].z) - mean; q += d * d; d = bfhi(xa[j].z) - mean; q += d * d; d = bflo(xa[j].w) - mean; q += d * d; d = bfhi(xa[j].w) - mean; q += d * d; }
        q += __shfl_xor(q, 1); q += __shfl_xor(q, 2); q += __shfl_xor(q, 4);
        const float rstd = 1.f / sqrtf(q * (1.f / D_) + 1e-5f);
#pragma unroll
        for (int j = 0; j < 10; ++j) {
            const int c = (c0 + 8 * j) * 8;
            f32x4 m0 = *(const f32x4*)(mod + 640 + c), m1 = *(const f32x4*)(mod + 640 + c + 4), s0 = *(const f32x4*)(mod + c), s1 = *(const f32x4*)(mod + c + 4);
            v4u u = xa[j], o;
            o.x = pk2((bflo(u.x) - mean) * rstd * (1.f + m0[0]) + s0[0], (bfhi(u.x) - mean) * rstd * (1.f + m0[1]) + s0[1]);
            o.y = pk2((bflo(u.y) - mean) * rstd * (1.f + m0[2]) + s0[2], (bfhi(u.y) - mean) * rstd * (1.f + m0[3]) + s0[3]);
            o.z = pk2((bflo(u.z) - mean) * rstd * (1.f + m1[0]) + s1[0], (bfhi(u.z) - mean) * rstd * (1.f + m1[1]) + s1[1]);
            o.w = pk2((bflo(u.w) - mean) * rstd * (1.f + m1[2]) + s1[2], (bfhi(u.w) - mean) * rstd * (1.f + m1[3]) + s1[3]);
            st16c(rw, (unsigned)OFF_H + (unsigned)((row * D_ + c) * 2), o);
        }
    }
}

constexpr int C_RES = 0;
constexpr int C_MEM = 122880;
constexpr int C_SP  = 133120;
constexpr int C_FLAG = 145408;
constexpr int C_LDS = 145408 + 64;

__device__ __forceinline__ int lds_flag_get(char* lds) { return *(volatile LAS int*)(LAS char*)(lds + C_FLAG); }
__device__ __forceinline__ void lds_flag_set(char* lds, int v) { *(volatile LAS int*)(LAS char*)(lds + C_FLAG) = v; }
__device__ __forceinline__ bool poll_mem(__amdgpu_buffer_rsrc_t rs, unsigned goff, char* ldst, int tid, gu32* ctl) {
    v4u v[3]; unsigned spins = 0; bool dead = false;
    while (true) {
        asm volatile("" ::: "memory");
        v[0] = ld16c(rs, goff + tid * 16); v[1] = ld16c(rs, goff + (tid + 256) * 16);
        if (tid < 128) v[2] = ld16c(rs, goff + (tid + 512) * 16); else v[2] = v[1];
        bool ok = okv(v[0]) && okv(v[1]) && okv(v[2]);
        if (ok) break;
        if (spin_dead(spins, ctl)) { dead = true; break; }
    }
    *(v4u*)(ldst + tid * 16) = v[0]; *(v4u*)(ldst + (tid + 256) * 16) = v[1];
    if (tid < 128) *(v4u*)(ldst + (tid + 512) * 16) = v[2];
    return dead;
}

template <int NR, int BT> __device__ __forceinline__ void stage192(char* ldst, const u16* __restrict__ gsrc, int tid) {
    if (tid < 192) {
        const int r0 = tid / 24, ch = tid % 24;
        const u16* gp = gsrc + (size_t)r0 * HE + ch * 8;
        const int o0 = r0 * 384 + (((ch >> 1) ^ ((r0 >> 1) & 1)) << 5) + ((ch & 1) << 4);
        const int o1 = r0 * 384 + (((ch >> 1) ^ (((r0 >> 1) & 1) | 2)) << 5) + ((ch & 1) << 4);
#pragma unroll 1
        for (int i0 = 0; i0 < NR / 8; i0 += BT) {
            v4u r[BT];
#pragma unroll
            for (int i = 0; i < BT; ++i) r[i] = *(const v4u*)(gp + (size_t)(i0 + i) * 8 * HE);
#pragma unroll
            for (int i = 0; i < BT; ++i) *(v4u*)(ldst + ((i & 1) ? o1 : o0) + (i0 + i) * 3072) = r[i];
        }
    }
}
__device__ __forceinline__ void stage640(char* ldst, const u16* __restrict__ gsrc, int tid) {
    const int r0 = tid >> 4, c0 = tid & 15;
    const int f = (r0 & 3) | (((r0 >> 3) & 1) << 2);
    const u16* gp = gsrc + (size_t)r0 * D_ + c0 * 8;
    const int o = r0 * 1280 + (((c0 >> 1) ^ f) << 5) + ((c0 & 1) << 4);
#pragma unroll 1
    for (int i = 0; i < 6; ++i) {
        v4u r[5];
#pragma unroll
        for (int j = 0; j < 5; ++j) r[j] = *(const v4u*)(gp + (size_t)i * 16 * D_ + j * 128);
#pragma unroll
        for (int j = 0; j < 5; ++j) *(v4u*)(ldst + o + i * 16 * 1280 + j * 256) = r[j];
    }
}

__device__ __forceinline__ void chain_qk(char* lds, int h, const u16* __restrict__ Wq, const u16* __restrict__ Wk, char* ws, __amdgpu_buffer_rsrc_t rs, __amdgpu_buffer_rsrc_t rw) {
    gu32* ctl = (gu32*)(ws + OFF_CTL);
    const int tid = threadIdx.x, lane = tid & 63, w = tid >> 6, g = lane >> 4, l15 = lane & 15;
    const int et0 = 3 * w;
    bf16x8 wq[3][15], wk[3][15];
#pragma unroll
    for (int c = 0; c < 3; ++c) {
        __syncthreads();
        stage192<160, 10>(lds, Wq + (size_t)(c * 160) * HE + h * E_, tid); stage192<160, 10>(lds + 61440, Wk + (size_t)(c * 160) * HE + h * E_, tid);
        __syncthreads();
#pragma unroll
        for (int k5 = 0; k5 < 5; ++k5)
#pragma unroll
            for (int j = 0; j < 3; ++j) { wq[j][c * 5 + k5] = trfrag192(lds, 32 * k5, et0 + j, lane); wk[j][c * 5 + k5] = trfrag192(lds + 61440, 32 * k5, et0 + j, lane); }
    }
    __syncthreads();
    stage192<160, 4>(lds + C_RES, Wq + (size_t)480 * HE + h * E_, tid); stage192<160, 4>(lds + C_RES + 61440, Wk + (size_t)480 * HE + h * E_, tid);
    const unsigned kg0 = (unsigned)OFF_K + (unsigned)((l15 * HE + h * E_ + 48 * w + 4 * g) * 2);
    v2u kx[2][3];
    wait_cnt(ctl, CW_A, 68);
#pragma unroll
    for (int kt = 0; kt < 2; ++kt)
#pragma unroll
        for (int j = 0; j < 3; ++j) kx[kt][j] = ld8c(rw, kg0 + (unsigned)((16 * kt) * HE * 2 + 32 * j));
    if (tid == 0) lds_flag_set(lds, 0);
    __syncthreads();
    bf16x8 la[6];
#pragma unroll
    for (int j = 0; j < 3; ++j) { la[2 * j] = trfrag192(lds + C_RES, 0, et0 + j, lane); la[2 * j + 1] = trfrag192(lds + C_RES + 61440, 0, et0 + j, lane); }
    const float scale = 0.07216878364870322f;
#pragma unroll 1
    for (int s = 0; s < NSEG; ++s) {
        ST(h, s, 0);
        poll_mem(rs, s * 10240, lds + C_MEM, tid, ctl);
        bar_lds();
        ST(h, s, 1);
        f32x4 aq[3], ak[3];
#pragma unroll
        for (int j = 0; j < 3; ++j) { aq[j] = (f32x4){0.f, 0.f, 0.f, 0.f}; ak[j] = (f32x4){0.f, 0.f, 0.f, 0.f}; }
        {
            const char* mb = lds + C_MEM + (g * 8 + (lane & 7)) * 16;
            bf16x8 b[20];
            b[0] = *(const bf16x8*)(mb + 0 * 512); b[1] = *(const bf16x8*)(mb + 1 * 512);
#pragma unroll
            for (int p = 0; p < 20; ++p) {
                const int ks = ((p & 3) == 3) ? 15 + (p >> 2) : 3 * (p >> 2) + (p & 3);
                if (p + 2 < 20) { const int k2 = (((p + 2) & 3) == 3) ? 15 + ((p + 2) >> 2) : 3 * ((p + 2) >> 2) + ((p + 2) & 3); b[p + 2] = *(const bf16x8*)(mb + k2 * 512); }
#pragma unroll
                for (int j = 0; j < 3; ++j) {
                    bf16x8 a0 = ((p & 3) != 3) ? wq[j][ks < 15 ? ks : 0] : la[2 * j];
                    aq[j] = mfma16(a0, b[p], aq[j]);
                    bf16x8 a1 = ((p & 3) != 3) ? wk[j][ks < 15 ? ks : 0] : la[2 * j + 1];
                    ak[j] = mfma16(a1, b[p], ak[j]);
                }
                SB();
                if ((p & 3) == 3) {
#pragma unroll
                    for (int j = 0; j < 3; ++j) { la[2 * j] = trfrag192(lds + C_RES, 32 * (((p >> 2) + 1) % 5), et0 + j, lane); la[2 * j + 1] = trfrag192(lds + C_RES + 61440, 32 * (((p >> 2) + 1) % 5), et0 + j, lane); }
                    SB();
                }
            }
        }
        v2u qp[3], kp[3];
#pragma unroll
        for (int j = 0; j < 3; ++j) { qp[j] = (v2u){pk2(aq[j][0], aq[j][1]), pk2(aq[j][2], aq[j][3])}; kp[j] = (v2u){pk2(ak[j][0], ak[j][1]), pk2(ak[j][2], ak[j][3])}; }
        if (s + 1 < NSEG) {
            bf16x8 q0 = __builtin_bit_cast(bf16x8, (v4u){qp[0].x, qp[0].y, qp[1].x, qp[1].y}), q1 = __builtin_bit_cast(bf16x8, (v4u){qp[2].x, qp[2].y, 0u, 0u});
            bf16x8 k0 = __builtin_bit_cast(bf16x8, (v4u){kp[0].x, kp[0].y, kp[1].x, kp[1].y}), k1 = __builtin_bit_cast(bf16x8, (v4u){kp[2].x, kp[2].y, 0u, 0u});
            f32x4 z = {0.f, 0.f, 0.f, 0.f};
            f32x4 s0 = mfma16(k1, q1, mfma16(k0, q0, z));
            f32x4 sx[2];
#pragma unroll
            for (int kt = 0; kt < 2; ++kt) {
                bf16x8 x0 = __builtin_bit_cast(bf16x8, (v4u){kx[kt][0].x, kx[kt][0].y, kx[kt][1].x, kx[kt][1].y}), x1 = __builtin_bit_cast(bf16x8, (v4u){kx[kt][2].x, kx[kt][2].y, 0u, 0u});
                sx[kt] = mfma16(x1, q1, mfma16(x0, q0, z));
            }
            f32x4* sp = (f32x4*)(lds + C_SP) + w * 192 + lane;
            sp[0] = s0; sp[64] = sx[0]; sp[128] = sx[1];
        }
        bar_lds();
        ST(h, s, 2);
        if (w == 0 && s + 1 < NSEG) {
            const f32x4* sp = (const f32x4*)(lds + C_SP) + (l15 >> 3) * 384 + lane;
            f32x4 sc[3], p0[3], p1[3];
#pragma unroll
            for (int a = 0; a < 3; ++a) { p0[a] = sp[a * 64]; p1[a] = sp[192 + a * 64]; }
#pragma unroll
            for (int a = 0; a < 3; ++a) {
                f32x4 t = p0[a] + p1[a];
#pragma unroll
                for (int r = 0; r < 4; ++r) sc[a][r] = t[r] + __uint_as_float(__builtin_amdgcn_mov_dpp(__float_as_uint(t[r]), 0x128, 0xf, 0xf, true));
            }
            const float sl2 = scale * 1.4426950408889634f;
            float mx = -3.0e38f;
#pragma unroll
            for (int r = 0; r < 4; ++r) { float t0 = sc[0][r] * sl2; sc[0][r] = (g < 2) ? t0 : -3.0e38f; sc[1][r] *= sl2; sc[2][r] *= sl2;
                mx = fmaxf(mx, fmaxf(sc[0][r], fmaxf(sc[1][r], sc[2][r]))); }
            mx = xmax32(xmax16(mx));
            float sum = 0.f;
#pragma unroll
            for (int r = 0; r < 4; ++r) { sc[0][r] = __builtin_amdgcn_exp2f(sc[0][r] - mx); sc[1][r] = __builtin_amdgcn_exp2f(sc[1][r] - mx); sc[2][r] = __builtin_amdgcn_exp2f(sc[2][r] - mx); sum += sc[0][r] + sc[1][r] + sc[2][r]; }
            sum = xsum32(xsum16(sum));
            const float inv = __builtin_amdgcn_rcpf(sum);
            {
                unsigned a0 = pk2(sc[0][0] * inv, sc[0][1] * inv), a1 = pk2(sc[0][2] * inv, sc[0][3] * inv);
                unsigned b0 = pk2(sc[1][0] * inv, sc[1][1] * inv), b1 = pk2(sc[1][2] * inv, sc[1][3] * inv);
                unsigned c0 = pk2(sc[2][0] * inv, sc[2][1] * inv), c1 = pk2(sc[2][2] * inv, sc[2][3] * inv);
                auto x0 = __builtin_amdgcn_permlane16_swap(b0, c0, false, false); auto x1 = __builtin_amdgcn_permlane16_swap(b1, c1, false, false);
                auto y0 = __builtin_amdgcn_permlane16_swap(a0, a0, false, false); auto y1 = __builtin_amdgcn_permlane16_swap(a1, a1, false, false);
                const unsigned pb = (unsigned)(OFF_P - OFF_MEM) + s * 20480 + h * 640 + l15 * 16;
                if (l15 < 8) {
                    st16c(rs, pb + (1 + (g >> 1) + 2 * (g & 1)) * 128, (v4u){x0[0], x1[0], x0[1], x1[1]});
                    if (g == 0) st16c(rs, pb, (v4u){y0[0], y1[0], y0[1], y1[1]});
                }
            }
        }
        ST(h, s, 3);
        {
            auto x0 = __builtin_amdgcn_permlane16_swap(kp[0].x, kp[1].x, false, false); auto x1 = __builtin_amdgcn_permlane16_swap(kp[0].y, kp[1].y, false, false);
            auto y0 = __builtin_amdgcn_permlane16_swap(kp[2].x, kp[2].x, false, false); auto y1 = __builtin_amdgcn_permlane16_swap(kp[2].y, kp[2].y, false, false);
            const unsigned kb = (unsigned)OFF_KM + (unsigned)((((s * 32 + h) * 8 + l15) * E_ + 48 * w + 8 * (g >> 1)) * 2);
            if (l15 < 8) {
                st16c(rw, kb + 32 * (g & 1), (v4u){x0[0], x1[0], x0[1], x1[1]});
                if ((g & 1) == 0) st16c(rw, kb + 64, (v4u){y0[0], y1[0], y0[1], y1[1]});
            }
        }
        if (s + 1 < NSEG) {
            if (((s + 1) & 3) == 0) wait_cnt(ctl, CW_A + 32 * ((s + 1) >> 2), 68);
#pragma unroll
            for (int kt = 0; kt < 2; ++kt)
#pragma unroll
                for (int j = 0; j < 3; ++j) kx[kt][j] = ld8c(rw, kg0 + (unsigned)(((s + 1) * 32 + 16 * kt) * HE * 2 + 32 * j));
        }
    }
}

__device__ __forceinline__ void chain_vo(char* lds, int h, const u16* __restrict__ Wv, const u16* __restrict__ Wo, char* ws, __amdgpu_buffer_rsrc_t rs) {
    gu32* ctl = (gu32*)(ws + OFF_CTL);
    const int tid = threadIdx.x, lane = tid & 63, w = tid >> 6, g = lane >> 4, l15 = lane & 15;
    const int et0 = 3 * w;
    bf16x8 wv[3][20], wo[10][3];
#pragma unroll
    for (int c = 0; c < 2; ++c) {
        __syncthreads();
        stage192<320, 10>(lds, Wv + (size_t)(c * 320) * HE + h * E_, tid);
        __syncthreads();
#pragma unroll
        for (int k = 0; k < 10; ++k)
#pragma unroll
            for (int j = 0; j < 3; ++j) wv[j][c * 10 + k] = trfrag192(lds, 32 * k, et0 + j, lane);
    }
    const u16* Woh = Wo + (size_t)h * E_ * D_;
    __syncthreads();
    stage640(lds, Woh, tid);
    __syncthreads();
#pragma unroll
    for (int n = 0; n < 10; ++n)
#pragma unroll
        for (int t = 0; t < 3; ++t) wo[n][t] = trfragP<1280>(lds, 32 * t, 10 * w + n, lane);
    __syncthreads();
    stage640(lds + C_RES, Woh + 96 * D_, tid);
    if (tid == 0) lds_flag_set(lds, 0);
    __syncthreads();
    bf16x8 lb[2][5];
#pragma unroll
    for (int n = 0; n < 5; ++n) { lb[0][n] = trfragP<1280>(lds + C_RES, 0, 10 * w + n, lane); lb[1][n] = trfragP<1280>(lds + C_RES, 0, 10 * w + 5 + n, lane); }
    char* vfr = lds + C_SP;
#pragma unroll 1
    for (int s = 0; s < NSEG; ++s) {
        if ((s & 3) == 0) wait_cnt(ctl, CW_A + 32 * (s >> 2), 68);
        ST(32 + h, s, 0);
        poll_mem(rs, s * 10240, lds + C_MEM, tid, ctl);
        bar_lds();
        ST(32 + h, s, 1);
        f32x4 acc[3] = {{0.f, 0.f, 0.f, 0.f}, {0.f, 0.f, 0.f, 0.f}, {0.f, 0.f, 0.f, 0.f}};
        {
            const char* mb = lds + C_MEM + (g * 8 + (lane & 7)) * 16;
            bf16x8 b[20];
#pragma unroll
            for (int p = 0; p < 4; ++p) b[p] = *(const bf16x8*)(mb + p * 512);
#pragma unroll
            for (int p = 0; p < 20; ++p) {
                if (p + 4 < 20) b[p + 4] = *(const bf16x8*)(mb + (p + 4) * 512);
#pragma unroll
                for (int j = 0; j < 3; ++j) acc[j] = mfma16(wv[j][p], b[p], acc[j]);
                SB();
            }
        }
        if (l15 < 8) {
#pragma unroll
            for (int j = 0; j < 3; ++j) *(v2u*)(vfr + ((6 * w + 2 * j + (g >> 1)) * 8 + l15) * 16 + (g & 1) * 8) = (v2u){pk2(acc[j][0], acc[j][1]), pk2(acc[j][2], acc[j][3])};
        }
        bar_lds();
        ST(32 + h, s, 2);
        f32x4 ua[10];
#pragma unroll
        for (int n = 0; n < 10; ++n) ua[n] = (f32x4){0.f, 0.f, 0.f, 0.f};
        {
            const char* vb = vfr + (g * 8 + (lane & 7)) * 16;
            bf16x8 a[6];
#pragma unroll
            for (int t = 0; t < 6; ++t) a[t] = *(const bf16x8*)(vb + t * 512);
#pragma unroll
            for (int k = 0; k < 6; ++k) {
#pragma unroll
                for (int n = 0; n < 5; ++n) ua[5 * (k & 1) + n] = mfma16(a[k >> 1], wo[5 * (k & 1) + n][k >> 1], ua[5 * (k & 1) + n]);
                SB();
#pragma unroll
                for (int n = 0; n < 5; ++n) ua[5 * (k & 1) + n] = mfma16(a[3 + (k >> 1)], lb[k & 1][n], ua[5 * (k & 1) + n]);
                SB();
#pragma unroll
                for (int n = 0; n < 5; ++n) lb[k & 1][n] = trfragP<1280>(lds + C_RES, 32 * (((k + 2) % 6) >> 1), 10 * w + 5 * (k & 1) + n, lane);
                SB();
            }
        }
        {
#pragma unroll
            for (int n = 0; n < 10; n += 2) {
                unsigned a0 = pk2(ua[n][0], ua[n][1]), a1 = pk2(ua[n][2], ua[n][3]), b0 = pk2(ua[n + 1][0], ua[n + 1][1]), b1 = pk2(ua[n + 1][2], ua[n + 1][3]);
                auto x0 = __builtin_amdgcn_permlane16_swap(a0, b0, false, false); auto x1 = __builtin_amdgcn_permlane16_swap(a1, b1, false, false);
                unsigned off = (unsigned)(OFF_UM - OFF_MEM) + ((s * 32 + h) * 40 + 10 * w + n + g) * 256 + l15 * 16;
                if (g < 2) st16c(rs, off, (v4u){x0[0], x1[0], x0[1], x1[1]});
            }
        }
        ST(32 + h, s, 3);
    }
}

__device__ __forceinline__ void chain_red(char* lds, int r, char* ws, __amdgpu_buffer_rsrc_t rs, __amdgpu_buffer_rsrc_t rw) {
    gu32* ctl = (gu32*)(ws + OFF_CTL);
    const int tid = threadIdx.x, lane = tid & 63, w = tid >> 6, g = lane >> 4, l15 = lane & 15;
    float* red = (float*)lds;
    if (tid == 0) lds_flag_set(lds, 0);
    __syncthreads();
    auto ldux = [&](int s, v4u (&ux)[10]) {
#pragma unroll
        for (int t = 0; t < 10; ++t) { int cgl = 4 * t + g, hh = cgl / 5, jg = cgl % 5; int jj = jg ? jg - 1 : 0;
            ux[t] = ld16c(rw, (unsigned)OFF_UX + (unsigned)(((((s * 32 + 8 * w + hh) * 40 + r) * 4 + jj) * 16 + l15) * 16)); }
    };
    v4u ux[10]; wait_cnt(ctl, CW_A, 68); ldux(0, ux);
#pragma unroll 1
    for (int s = 0; s + 1 < NSEG; ++s) {
        v4u pf[10], um0, um1; unsigned spins = 0; bool dead = false;
        ST(64 + r, s, 0);
        const unsigned pb = (unsigned)(OFF_P - OFF_MEM) + s * 20480 + ((40 * w + g) * 8 + (lane & 7)) * 16;
        const unsigned ub = (unsigned)(OFF_UM - OFF_MEM) + ((s * 32 + 8 * w + g) * 40 + r) * 256 + l15 * 16;
        const unsigned pd = (unsigned)(OFF_P - OFF_MEM) + s * 20480 + ((40 * w + 4 * (l15 >> 3) + g) * 8 + (lane & 7)) * 16;
        v4u pr[5];
        while (true) {
            asm volatile("" ::: "memory");
            um0 = ld16c(rs, ub); um1 = ld16c(rs, ub + 4 * 40 * 256);
            if (__all(okv(um0) && okv(um1))) break;
            if (spin_dead(spins, ctl)) { dead = true; break; }
        }
        while (true) {
            asm volatile("" ::: "memory");
#pragma unroll
            for (int t = 0; t < 5; ++t) pr[t] = ld16c(rs, pd + t * 1024);
            bool ok = true;
#pragma unroll
            for (int t = 0; t < 5; ++t) ok &= okv(pr[t]);
            if (__all(ok)) break;
            if (spin_dead(spins, ctl)) { dead = true; break; }
        }
#pragma unroll
        for (int t = 0; t < 5; ++t) {
            pf[2 * t] = pr[t];
            pf[2 * t + 1] = (v4u){(unsigned)__builtin_amdgcn_mov_dpp(pr[t].x, 0x128, 0xf, 0xf, true), (unsigned)__builtin_amdgcn_mov_dpp(pr[t].y, 0x128, 0xf, 0xf, true),
                                  (unsigned)__builtin_amdgcn_mov_dpp(pr[t].z, 0x128, 0xf, 0xf, true), (unsigned)__builtin_amdgcn_mov_dpp(pr[t].w, 0x128, 0xf, 0xf, true)};
        }
        ST(64 + r, s, 1);
        f32x4 acc0 = {0.f, 0.f, 0.f, 0.f}, acc1 = {0.f, 0.f, 0.f, 0.f};
#pragma unroll
        for (int t = 0; t < 5; ++t) {
            v4u a = (t < 4 && g == t) ? um0 : ux[t];
            acc0 = mfma16(__builtin_bit_cast(bf16x8, a), __builtin_bit_cast(bf16x8, pf[t]), acc0);
            v4u b = (t < 4 && g == t) ? um1 : ux[5 + t];
            acc1 = mfma16(__builtin_bit_cast(bf16x8, b), __builtin_bit_cast(bf16x8, pf[5 + t]), acc1);
        }
        acc0 += acc1;
        if (l15 < 8) *(f32x4*)(red + (w * 8 + l15) * 16 + 4 * g) = acc0;
        bar_lds();
        ST(64 + r, s, 2);
        if (w == 0) {
            int i = lane >> 3, cp = lane & 7; float a0 = 0.f, a1 = 0.f;
#pragma unroll
            for (int q = 0; q < 4; ++q) { float2 p = *(const float2*)(red + (q * 8 + i) * 16 + 2 * cp); a0 += p.x; a1 += p.y; }
            unsigned d0 = pk2(a0, a1);
            unsigned d1 = __builtin_amdgcn_mov_dpp(d0, 0x55, 0xf, 0xf, true), d2 = __builtin_amdgcn_mov_dpp(d0, 0xAA, 0xf, 0xf, true), d3 = __builtin_amdgcn_mov_dpp(d0, 0xFF, 0xf, 0xf, true);
            if ((cp & 3) == 0) st16c(rs, (s + 1) * 10240 + r * 256 + (cp >> 2) * 128 + i * 16, (v4u){d0, d1, d2, d3});
        }
        ST(64 + r, s, 3);
        if (s + 2 < NSEG) { if (((s + 1) & 3) == 0) wait_cnt(ctl, CW_A + 32 * ((s + 1) >> 2), 68); ldux(s + 1, ux); }
        bar_lds();
    }
}

struct Params { const u16 *x, *Wq, *Wk, *Wv, *Wo, *w1, *w3, *w2; char* ws; u16* out; };
__global__ void __launch_bounds__(256, 1) k_main(Params p) {
    extern __shared__ __attribute__((aligned(16))) char lds[];
    const int tid = threadIdx.x;
#define MAPM 1
    int b;
    { const int bx = blockIdx.x, xc = bx & 7, sl = bx >> 3;
      if (MAPM == 0) b = bx;
      else if (MAPM == 1) { int id = (xc < 4) ? sl * 4 + xc : 128 + sl * 4 + (xc - 4); b = id; }
      else if (MAPM == 2) { b = (xc == 0) ? sl : (xc == 1) ? 32 + sl : (xc < 4 && sl < 20) ? 64 + (xc - 2) * 20 + sl : -1;
             if (b < 0) { b = 104 + ((xc < 4) ? (xc - 2) * 12 + (sl - 20) : 24 + (xc - 4) * 32 + sl); } }
      else {   b = (xc < 2) ? (sl * 2 + xc) : (xc < 4 && sl < 20) ? 64 + (xc - 2) * 20 + sl : -1;
             if (b < 0) { b = 104 + ((xc < 4) ? (xc - 2) * 12 + (sl - 20) : 24 + (xc - 4) * 32 + sl); } } }
    char* ws = p.ws;
    const __amdgpu_buffer_rsrc_t rs = __builtin_amdgcn_make_buffer_rsrc(ws + OFF_MEM, 0, (int)(OFF_PT - OFF_MEM), 0x00020000);
    const __amdgpu_buffer_rsrc_t rw = __builtin_amdgcn_make_buffer_rsrc(ws, 0, (int)OFF_END, 0x00020000);
    if (b < 32) { chain_qk(lds, b, p.Wq, p.Wk, ws, rs, rw); return; }
    if (b < 64) { chain_vo(lds, b - 32, p.Wv, p.Wo, ws, rs); return; }
    if (b < 104) { chain_red(lds, b - 64, ws, rs, rw); return; }
    gu32* ctl = (gu32*)(ws + OFF_CTL);
    const int* tab = g_tasks.v;
    volatile LAS int* tslot = (volatile LAS int*)(LAS char*)(lds + C_FLAG);
#pragma unroll 1
    while (true) {
        __syncthreads();
        if (tid == 0) { int t = (int)__hip_atomic_fetch_add(ctl + CW_QUEUE, 1u, RLX_AGENT); tslot[0] = t; tslot[1] = t < NTASK_ALL ? tab[t] : -1; }
        __syncthreads();
        const int t = __builtin_amdgcn_readfirstlane(tslot[0]);
        if (t >= NTASK_ALL) break;
        const int code = __builtin_amdgcn_readfirstlane(tslot[1]), ty = code >> 16, ta = (code >> 8) & 255, tb = code & 255;
        int sig = -1;
#define TS1() do {} while (0)
        if (ty == 8) {
            TS1(); taskFill(ta, rw); sig = CW_A + 32 * (ta >> 2);
        } else if (ty == 9) {
            TS1(); taskLN(ta, p.x, ws, rw); sig = CW_LN + 32 * (ta >> 2);
        } else if (ty == 0) {
            if (tid < 64) { wait_cnt(ctl, CW_LN + 32 * ta, 4); if (ta > 0) wait_cnt(ctl, CW_A, 68); }
            __syncthreads();
            TS1(); taskA(lds, ta, tb, 2, p.Wq, p.Wk, p.Wv, p.Wo, ws, rw); sig = CW_A + 32 * ta;
        } else if (ty == 10) {
            if (tid < 64) { wait_cnt(ctl, CW_LN + 32 * ta, 4); if (ta > 0) wait_cnt(ctl, CW_A, 68); }
            __syncthreads();
            TS1(); taskA(lds, ta, tb, 0, p.Wq, p.Wk, p.Wv, p.Wo, ws, rw); sig = CW_A + 32 * ta;
        } else if (ty == 1) {
            if (tid < 64) wait_cnt(ctl, CW_LN + 32 * ta, 4);
            __syncthreads();
            TS1(); taskA(lds, ta, tb, 1, p.Wq, p.Wk, p.Wv, p.Wo, ws, rw); sig = CW_Q + 32 * ta;
        } else if (ty == 2) {
            if (tid < 64) { wait_cnt(ctl, CW_A + 32 * (ta >> 2), 68); wait_cnt(ctl, CW_Q + 32 * (ta >> 2), 32); }
            __syncthreads();
            TS1(); taskP(ta, tb, ws, rw); sig = CW_P + 32 * ta;
        } else if (ty == 3) {
            TS1(); taskO(lds, ta, tb, p.x, ws, rw, ctl); sig = CW_X + 32 * ta;
        } else if (ty == 4) {
            TS1(); taskF1(lds, ta, tb, p.w1, p.w3, ws, rw, ctl); sig = CW_F + 32 * ta;
        } else if (ty == 5) {
            TS1(); taskF2(lds, ta, tb, p.w2, ws, p.out, rw, ctl);
        } else if (ty == 6) {
            if (tid < 64) { for (int j = 0; j < 4; ++j) wait_cnt(ctl, CW_X + 32 * (4 * ta + j), 10); }
            __syncthreads();
            TS1(); taskF1B(lds, ta, tb, p.w1, p.w3, ws, rw); sig = CW_F + 32 * (4 * ta);
        } else {
            if (tid < 64) wait_cnt(ctl, CW_F + 32 * (4 * ta), 16);
            __syncthreads();
            TS1(); taskF2B(lds, ta, tb, p.w2, ws, p.out, rw);
        }
        drain_vm();
        __syncthreads();
        if (tid == 0 && sig >= 0) __hip_atomic_fetch_add(ctl + sig, 1u, RLX_AGENT);
    }
}

extern "C" void kernel_launch(void* const* d_in, const int* in_sizes, int n_in, void* d_out, int out_size, void* d_ws, size_t ws_size, hipStream_t stream) {
    const u16* x = (const u16*)d_in[0]; const u16* mem0 = (const u16*)d_in[1];
    const u16* c = (const u16*)d_in[11]; const u16* wmod = (const u16*)d_in[12];
    char* ws = (char*)d_ws;
    Params p; p.x = x; p.Wq = (const u16*)d_in[2]; p.Wk = (const u16*)d_in[3]; p.Wv = (const u16*)d_in[4]; p.Wo = (const u16*)d_in[5];
    p.w1 = (const u16*)d_in[8]; p.w3 = (const u16*)d_in[9]; p.w2 = (const u16*)d_in[10]; p.ws = ws; p.out = (u16*)d_out;
    (void)hipFuncSetAttribute((const void*)k_main, hipFuncAttributeMaxDynamicSharedMemorySize, C_LDS);
    k_pre<<<121, 256, 0, stream>>>(c, wmod, mem0, ws);
    k_main<<<256, 256, C_LDS, stream>>>(p);
}
```

```cpp
#include <hip/hip_runtime.h>
#include <hip/hip_bf16.h>
#include <stdint.h>

#define LAS __attribute__((address_space(3)))
#define GAS __attribute__((address_space(1)))
typedef short bf16x8 __attribute__((ext_vector_type(8)));
typedef short bf16x4 __attribute__((ext_vector_type(4)));
typedef float f32x4 __attribute__((ext_vector_type(4)));
typedef unsigned v4u __attribute__((ext_vector_type(4)));
typedef unsigned v2u __attribute__((ext_vector_type(2)));
typedef unsigned short u16;
typedef GAS unsigned gu32;
#define RLX_AGENT __ATOMIC_RELAXED, __HIP_MEMORY_SCOPE_AGENT

constexpr int S_ = 2048, D_ = 640, H_ = 32, E_ = 192, HE = 6144, NSEG = 64, F_ = 1536;
constexpr size_t OFF_MOD = 0;
constexpr size_t OFF_CTL = 16384;
constexpr size_t OFF_TAB = 49152;
constexpr size_t OFF_H   = 65536;
constexpr size_t OFF_Q   = OFF_H + (size_t)S_ * D_ * 2;
constexpr size_t OFF_K   = OFF_Q + (size_t)S_ * HE * 2;
constexpr size_t OFF_UX  = OFF_K + (size_t)S_ * HE * 2;
constexpr size_t OFF_MEM = OFF_UX + (size_t)NSEG * H_ * 40 * 4 * 256;
constexpr size_t OFF_P   = OFF_MEM + (size_t)65 * 10240;
constexpr size_t OFF_UM  = OFF_P + (size_t)NSEG * 20480;
constexpr size_t OFF_KM  = OFF_UM + (size_t)NSEG * H_ * 40 * 256;
constexpr size_t OFF_PT  = OFF_KM + (size_t)NSEG * H_ * 8 * E_ * 2;
constexpr size_t OFF_X1  = OFF_PT + (size_t)S_ * 1280 * 2;
constexpr size_t OFF_ACT = OFF_X1 + (size_t)S_ * D_ * 2;
constexpr size_t OFF_END = OFF_ACT + (size_t)S_ * F_ * 2;
constexpr unsigned SENT = 0xFFFFFFFFu;
constexpr unsigned kSpinCap = 1u << 20;
constexpr int CW_QUEUE = 0, CW_TMO = 32, CW_A = 64  , CW_Q = CW_A + 32 * 16  , CW_P = CW_Q + 32 * 16  , CW_X = CW_P + 32 * 64, CW_F = CW_X + 32 * 64, CW_FILL = CW_F + 32 * 64, CW_LN = CW_FILL + 32, CW_END = CW_LN + 32 * 16;
static_assert(CW_END * 4 <= 32768, "ctl");
#define ST(role_idx, s_, k_) do {} while (0)

__device__ __forceinline__ float bf2f(u16 v) { return __uint_as_float((unsigned)v << 16); }
__device__ __forceinline__ float bflo(unsigned v) { return __uint_as_float(v << 16); }
__device__ __forceinline__ float bfhi(unsigned v) { return __uint_as_float(v & 0xFFFF0000u); }
typedef __bf16 nbf2 __attribute__((ext_vector_type(2)));
__device__ __forceinline__ unsigned pk2(float a, float b) { nbf2 t = {(__bf16)a, (__bf16)b}; return __builtin_bit_cast(unsigned, t); }
__device__ __forceinline__ float rbf(float a) { return bflo(pk2(a, 0.f)); }
__device__ __forceinline__ f32x4 mfma16(bf16x8 a, bf16x8 b, f32x4 c) { return __builtin_amdgcn_mfma_f32_16x16x32_bf16(a, b, c, 0, 0, 0); }
__device__ __forceinline__ bf16x4 trread(const char* lds_base, int off) { return __builtin_amdgcn_ds_read_tr16_b64_v4i16((LAS bf16x4*)(lds_base + off)); }
__device__ __forceinline__ bf16x8 cat8(bf16x4 a, bf16x4 b) { return __builtin_shufflevector(a, b, 0, 1, 2, 3, 4, 5, 6, 7); }
__device__ __forceinline__ v4u ld16c(__amdgpu_buffer_rsrc_t r, unsigned off) { return __builtin_amdgcn_raw_buffer_load_b128(r, off, 0, 16); }
__device__ __forceinline__ v2u ld8c(__amdgpu_buffer_rsrc_t r, unsigned off) { return __builtin_amdgcn_raw_buffer_load_b64(r, off, 0, 16); }
__device__ __forceinline__ void st16c(__amdgpu_buffer_rsrc_t r, unsigned off, v4u v) { __builtin_amdgcn_raw_buffer_store_b128(v, r, off, 0, 16); }
__device__ __forceinline__ bool okv(v4u v) { return v.x != SENT && v.y != SENT && v.z != SENT && v.w != SENT; }
__device__ __forceinline__ bf16x8 asf(v4u v) { return __builtin_bit_cast(bf16x8, v); }
#define SB() __builtin_amdgcn_sched_barrier(0)
__device__ __forceinline__ float xmax16(float x) { unsigned u = __float_as_uint(x); auto r = __builtin_amdgcn_permlane16_swap(u, u, false, false); return fmaxf(__uint_as_float(r[0]), __uint_as_float(r[1])); }
__device__ __forceinline__ float xmax32(float x) { unsigned u = __float_as_uint(x); auto r = __builtin_amdgcn_permlane32_swap(u, u, false, false); return fmaxf(__uint_as_float(r[0]), __uint_as_float(r[1])); }
__device__ __forceinline__ float xsum16(float x) { unsigned u = __float_as_uint(x); auto r = __builtin_amdgcn_permlane16_swap(u, u, false, false); return __uint_as_float(r[0]) + __uint_as_float(r[1]); }
__device__ __forceinline__ float xsum32(float x) { unsigned u = __float_as_uint(x); auto r = __builtin_amdgcn_permlane32_swap(u, u, false, false); return __uint_as_float(r[0]) + __uint_as_float(r[1]); }
__device__ __forceinline__ v4u pair16(f32x4 a, f32x4 b) {
    unsigned a0 = pk2(a[0], a[1]), a1 = pk2(a[2], a[3]), b0 = pk2(b[0], b[1]), b1 = pk2(b[2], b[3]);
    auto x0 = __builtin_amdgcn_permlane16_swap(a0, b0, false, false); auto x1 = __builtin_amdgcn_permlane16_swap(a1, b1, false, false);
    return (v4u){x0[0], x1[0], x0[1], x1[1]};
}
__device__ __forceinline__ void bar_lds() { asm volatile("s_waitcnt lgkmcnt(0)\n\ts_barrier" ::: "memory"); }
__device__ __forceinline__ void drain_vm() { asm volatile("s_waitcnt vmcnt(0)" ::: "memory"); }
__device__ __forceinline__ bool wait_cnt(gu32* ctl, int idx, unsigned target) {
    for (unsigned spins = 0;; ++spins) {
        asm volatile("" ::: "memory");
        unsigned v = __builtin_amdgcn_readfirstlane(__hip_atomic_load(ctl + idx, RLX_AGENT));
        if (v >= target) return false;
        if (spins > kSpinCap) { __hip_atomic_store(ctl + CW_TMO, 1u, RLX_AGENT); return true; }
        if ((spins & 63u) == 63u && __builtin_amdgcn_readfirstlane(__hip_atomic_load(ctl + CW_TMO, RLX_AGENT))) return true;
        __builtin_amdgcn_s_sleep(2);
    }
}

__device__ __forceinline__ bool spin_dead(unsigned& spins, gu32* ctl) {
    ++spins;
    if (spins > kSpinCap) { __hip_atomic_store(ctl + CW_TMO, 1u, RLX_AGENT); return true; }
    if ((spins & 1023u) == 1023u) return __builtin_amdgcn_readfirstlane(__hip_atomic_load(ctl + CW_TMO, RLX_AGENT)) != 0;
    return false;
}

__device__ __forceinline__ int boff192(int row, int ch) { int f = ((row >> 1) & 1) | (((row >> 3) & 1) << 1); return row * 384 + (((ch >> 1) ^ f) << 5) + ((ch & 1) << 4); }
template <int RB> __device__ __forceinline__ int boffP(int row, int ch) { int f = (row & 3) | (((row >> 3) & 1) << 2); return row * RB + (((ch >> 1) ^ f) << 5) + ((ch & 1) << 4); }
__device__ __forceinline__ bf16x8 trfrag192(const char* base, int r0, int c, int lane) {
    const int g = lane >> 4, q = (lane & 15) >> 2, p = lane & 3;
    const int f = ((q >> 1) & 1) | ((g & 1) << 1);
    const int a = (r0 + 8 * g + q) * 384 + ((c ^ f) << 5) + 8 * p;
    return cat8(trread(base, a), trread(base, a + 4 * 384));
}
template <int RB> __device__ __forceinline__ bf16x8 trfragP(const char* base, int r0, int c, int lane) {
    const int g = lane >> 4, q = (lane & 15) >> 2, p = lane & 3;
    const int f = q | ((g & 1) << 2);
    const int a = (r0 + 8 * g + q) * RB + ((c ^ f) << 5) + 8 * p;
    return cat8(trread(base, a), trread(base, a + 4 * RB));
}

#define SK0V 52
constexpr int SK0 = SK0V;
__host__ __device__ constexpr int slot_size(int t) {
    int n = 0;
    if (t + 2 < 64) n += 4;
    if (t == 0) n += 8;
    if (t >= 1 && t <= 64) n += 10;
    { int s = t - 2; if (s >= 0 && s < SK0) n += (s % 4 == 3) ? 16 : 0; else if (s >= SK0 && s < 64) n += 32; }
    { int s = t - 3; if (s >= 0 && s < SK0) n += (s % 4 == 3) ? 4 : 0; else if (s >= SK0 && s < 64) n += 10; }
    return n;
}
__host__ __device__ constexpr int slot_task(int t, int v) {
    if (t == 0) { if (v < 8) return (2 << 16) | ((v >> 2) << 8) | (v & 3); v -= 8; }
    if (t + 2 < 64) { if (v < 4) return (2 << 16) | ((t + 2) << 8) | v; v -= 4; }
    if (t >= 1 && t <= 64) { if (v < 10) return (3 << 16) | ((t - 1) << 8) | v; v -= 10; }
    { int s = t - 2; if (s >= 0 && s < SK0) { if (s % 4 == 3) { if (v < 16) return (6 << 16) | ((s >> 2) << 8) | v; v -= 16; } } else if (s >= SK0 && s < 64) { if (v < 32) return (4 << 16) | (s << 8) | v; v -= 32; } }
    { int s = t - 3; if (s >= 0 && s < SK0) { if (s % 4 == 3) return (7 << 16) | ((s >> 2) << 8) | v; } else return (5 << 16) | (s << 8) | v; }
    return -1;
}
__host__ __device__ constexpr int group_size(int r) {
    int n = 0; if (r < 16) n += 64; if (r - 1 >= 0 && r - 1 < 16) n += 32;
    for (int j = 0; j < 4; ++j) { int t = 4 * (r - 4) + j; if (t >= 0 && t < 67) n += slot_size(t); }
    return n;
}
__host__ __device__ constexpr int total_tasks() { int n = 0; for (int r = 0; r < 22; ++r) n += group_size(r); return n; }
constexpr int NTASK = total_tasks();
static_assert(NTASK <= 4096, "task table");
constexpr int NPRE = 64 + 64;
constexpr int NTASK_ALL = NTASK + NPRE;
struct TaskTab { int v[NTASK_ALL]; };
constexpr TaskTab make_tasks() {
    TaskTab T{}; int pos = 0;
    for (int i = 0; i < 4; ++i) T.v[pos++] = (8 << 16) | (i << 8);
    for (int i = 0; i < 64; ++i) T.v[pos++] = (9 << 16) | (i << 8);
    for (int r = 0; r < 22; ++r) {
        if (r + 1 < 16) { for (int i = 0; i < 4; ++i) T.v[pos++] = (8 << 16) | ((4 * (r + 1) + i) << 8); }
        if (r < 16) { for (int h = 0; h < 32; ++h) T.v[pos++] = (0 << 16) | (r << 8) | h; for (int h = 0; h < 32; ++h) T.v[pos++] = (10 << 16) | (r << 8) | h; }
        if (r - 1 >= 0 && r - 1 < 16) { for (int h = 0; h < 32; ++h) T.v[pos++] = (1 << 16) | ((r - 1) << 8) | h; }
        for (int j = 0; j < 4; ++j) { int t = 4 * (r - 4) + j; if (t >= 0 && t < 67) { int n = slot_size(t); for (int q = 0; q < n; ++q) T.v[pos++] = slot_task(t, q); } }
    }
    return T;
}
__device__ const TaskTab g_tasks = make_tasks();

__global__ void __launch_bounds__(256) k_pre(const u16* __restrict__ c, const u16* __restrict__ wmod, const u16* __restrict__ mem0, char* ws) {
    __shared__ float sc[640]; __shared__ float red[64][33];
    const int b = blockIdx.x, tid = threadIdx.x;
    if (b < 120) {
        for (int i = tid; i < 640; i += 256) { float v = bf2f(c[i]); sc[i] = rbf(v / (1.f + __expf(-v))); }
        __syncthreads();
        const int kg = tid >> 2, cq = tid & 3;
        v4u wv[10];
#pragma unroll
        for (int j = 0; j < 10; ++j) wv[j] = *(const v4u*)(wmod + (size_t)(kg * 10 + j) * 3840 + b * 32 + cq * 8);
        float a[8] = {0.f, 0.f, 0.f, 0.f, 0.f, 0.f, 0.f, 0.f};
#pragma unroll
        for (int j = 0; j < 10; ++j) { float s = sc[kg * 10 + j];
            a[0] += s * bflo(wv[j].x); a[1] += s * bfhi(wv[j].x); a[2] += s * bflo(wv[j].y); a[3] += s * bfhi(wv[j].y);
            a[4] += s * bflo(wv[j].z); a[5] += s * bfhi(wv[j].z); a[6] += s * bflo(wv[j].w); a[7] += s * bfhi(wv[j].w); }
#pragma unroll
        for (int j = 0; j < 8; ++j) red[kg][cq * 8 + j] = a[j];
        __syncthreads();
        if (tid < 32) { float s = 0.f; for (int k = 0; k < 64; ++k) s += red[k][tid]; ((float*)(ws + OFF_MOD))[b * 32 + tid] = rbf(s); }
    } else if (b == 120) {
        for (int ci = tid; ci < 640; ci += 256) { int i = ci & 7, kg = ci >> 3; *(v4u*)(ws + OFF_MEM + ci * 16) = *(const v4u*)(mem0 + i * 640 + kg * 8); }
        for (int i = tid; i < 8192; i += 256) ((unsigned*)(ws + OFF_CTL))[i] = 0;
    }
}

constexpr int GA_B = 128 * 128;
constexpr int GB_B = 64 * 384;
constexpr int GBUF = GA_B + GB_B;
#define PFD 2
#define PFD2 2
template <int NK, int PD, int AUXA, class AT>
__device__ __forceinline__ void gemm4(char* lds, __amdgpu_buffer_rsrc_t rsA, unsigned vA, unsigned sA, unsigned lda2,
                                      __amdgpu_buffer_rsrc_t rsB0, __amdgpu_buffer_rsrc_t rsB1, unsigned vB0, unsigned vB1, unsigned sB, unsigned ldb2, int cB0, int cB1,
                                      AT atr, f32x4 (&acc)[4][6]) {
    int tid = threadIdx.x; asm volatile("" : "+v"(tid));
    const int lane = tid & 63, w = tid >> 6, wm = w >> 1, wn = w & 1, g = lane >> 4, l15 = lane & 15;
#pragma unroll
    for (int a = 0; a < 4; ++a)
#pragma unroll
        for (int b = 0; b < 6; ++b) acc[a][b] = (f32x4){0.f, 0.f, 0.f, 0.f};
    v4u ra[PD][4], rb[PD][8];
    const int ar = tid >> 3, ach = tid & 7, br = (tid < 192 ? tid : 0) / 12;
    const int bf = ((br >> 1) & 1) | (((br >> 3) & 1) << 1);
    const int bo0 = GA_B + br * 384 + (((cB0 >> 1) ^ bf) << 5) + ((cB0 & 1) << 4);
    const int bo1 = GA_B + br * 384 + (((cB1 >> 1) ^ bf) << 5) + ((cB1 & 1) << 4);
    const int ao = ar * 128 + ((ach ^ (ar & 7)) << 4);
    auto gload = [&](int kc, v4u (&a4)[4], v4u (&b8)[8]) {
#pragma unroll
        for (int i = 0; i < 4; ++i) a4[i] = __builtin_amdgcn_raw_buffer_load_b128(rsA, vA, sA + kc * 128 + i * 32 * lda2, AUXA);
#pragma unroll
        for (int i = 0; i < 4; ++i) {
            b8[i] = __builtin_amdgcn_raw_buffer_load_b128(rsB0, vB0, sB + (kc * 64 + 16 * i) * ldb2, 0);
            b8[4 + i] = __builtin_amdgcn_raw_buffer_load_b128(rsB1, vB1, sB + (kc * 64 + 16 * i) * ldb2, 0);
        }
    };
    auto lstore = [&](int kc, char* buf, v4u (&a4)[4], v4u (&b8)[8]) {
#pragma unroll
        for (int i = 0; i < 4; ++i) *(v4u*)(buf + ao + i * 4096) = atr(kc, ar + 32 * i, ach, a4[i]);
        if (tid < 192) {
#pragma unroll
            for (int i = 0; i < 4; ++i) { *(v4u*)(buf + bo0 + i * 6144) = b8[i]; *(v4u*)(buf + bo1 + i * 6144) = b8[4 + i]; }
        }
    };
#pragma unroll
    for (int k = 0; k < PD; ++k) gload(k, ra[k], rb[k]);
    lstore(0, lds, ra[0], rb[0]);
    if (PD < NK) gload(PD, ra[0], rb[0]);
    __syncthreads();
#pragma unroll 1
    for (int k0 = 0; k0 < NK; k0 += PD) {
#pragma unroll
        for (int u = 0; u < PD; ++u) {
            const int kc = k0 + u;
            char* cur = lds + (kc & 1) * GBUF; char* nxt = lds + ((kc + 1) & 1) * GBUF;
#pragma unroll
            for (int ks = 0; ks < 2; ++ks) {
                bf16x8 af[4], bfr[6];
#pragma unroll
                for (int mt = 0; mt < 4; ++mt) { int r = wm * 64 + mt * 16 + l15; af[mt] = *(const bf16x8*)(cur + r * 128 + (((4 * ks + g) ^ (r & 7)) << 4)); }
#pragma unroll
                for (int nt = 0; nt < 6; ++nt) bfr[nt] = trfrag192(cur + GA_B, 32 * ks, wn * 6 + nt, lane);
#pragma unroll
                for (int mt = 0; mt < 4; ++mt)
#pragma unroll
                    for (int nt = 0; nt < 6; ++nt) acc[mt][nt] = mfma16(bfr[nt], af[mt], acc[mt][nt]);
            }
            if (kc + 1 < NK) {
                lstore(kc + 1, nxt, ra[(u + 1) % PD], rb[(u + 1) % PD]);
                if (kc + 1 + PD < NK) gload(kc + 1 + PD, ra[(u + 1) % PD], rb[(u + 1) % PD]);
            }
            __syncthreads();
        }
    }
}
__device__ __forceinline__ __amdgpu_buffer_rsrc_t mkrs(const void* p, unsigned bytes) { return __builtin_amdgcn_make_buffer_rsrc((void*)p, 0, (int)bytes, 0x00020000); }

constexpr int KA_VOFF = 2 * GBUF;
constexpr int KA_VSTR = 400;
__device__ __forceinline__ void taskA(char* lds, int rbk, int h, int qonly, const u16* __restrict__ Wq, const u16* __restrict__ Wk, const u16* __restrict__ Wv,
                                      const u16* __restrict__ Wo, char* ws, __amdgpu_buffer_rsrc_t rw) {
    int tid = threadIdx.x; asm volatile("" : "+v"(tid));
    const int lane = tid & 63, w = tid >> 6, wm = w >> 1, wn = w & 1, g = lane >> 4, l15 = lane & 15;
    f32x4 acc[4][6];
#pragma unroll 1
    for (int mat = qonly; mat <= qonly; ++mat) {
        const __amdgpu_buffer_rsrc_t rW = mkrs(mat == 0 ? Wk : (mat == 1 ? Wq : Wv), (unsigned)(D_ * HE * 2));
        const int bt = tid < 192 ? tid : 0, brr = bt / 12, bc = bt % 12;
        gemm4<10, PFD, 16>(lds, rw, (unsigned)(((tid >> 3) * D_ + (tid & 7) * 8) * 2), (unsigned)OFF_H + (unsigned)(rbk * 128 * D_ * 2), D_ * 2,
                          rW, rW, (unsigned)((brr * HE + bc * 8) * 2), (unsigned)((brr * HE + (12 + bc) * 8) * 2), (unsigned)(h * E_ * 2), HE * 2, bc, 12 + bc,
                          [](int, int, int, v4u v) { return v; }, acc);
        if (mat < 2) {
            const unsigned ob = (unsigned)(mat == 0 ? OFF_K : OFF_Q);
#pragma unroll
            for (int mt = 0; mt < 4; ++mt)
#pragma unroll
                for (int nt = 0; nt < 6; nt += 2) {
                    int tok = rbk * 128 + wm * 64 + mt * 16 + l15, e = wn * 96 + (nt + (g & 1)) * 16 + 8 * (g >> 1);
                    st16c(rw, ob + (unsigned)(tok * HE + h * E_ + e) * 2u, pair16(acc[mt][nt], acc[mt][nt + 1]));
                }
        } else {
#pragma unroll
            for (int mt = 0; mt < 4; ++mt)
#pragma unroll
                for (int nt = 0; nt < 6; ++nt) {
                    int tok = wm * 64 + mt * 16 + l15, e = wn * 96 + nt * 16 + 4 * g;
                    *(v2u*)(lds + KA_VOFF + tok * KA_VSTR + e * 2) = (v2u){pk2(acc[mt][nt][0], acc[mt][nt][1]), pk2(acc[mt][nt][2], acc[mt][nt][3])};
                }
        }
    }
    if (qonly != 2) return;
    __syncthreads();
    bf16x8 vf[4][6];
#pragma unroll
    for (int mt = 0; mt < 4; ++mt)
#pragma unroll
        for (int t = 0; t < 6; ++t) vf[mt][t] = *(const bf16x8*)(lds + KA_VOFF + (wm * 64 + mt * 16 + l15) * KA_VSTR + (4 * t + g) * 16);
    const __amdgpu_buffer_rsrc_t rWo = mkrs(Wo, (unsigned)(HE * D_ * 2));
    const int r0 = tid >> 4, c0 = tid & 15;
    const int wof = r0 * 256 + (((c0 >> 1) ^ ((r0 & 3) | (((r0 >> 3) & 1) << 2))) << 5) + ((c0 & 1) << 4);
    v4u rb[12];
    auto gl = [&](int nc) {
#pragma unroll
        for (int i = 0; i < 12; ++i) rb[i] = __builtin_amdgcn_raw_buffer_load_b128(rWo, (unsigned)((r0 * D_ + c0 * 8) * 2), (unsigned)(((h * E_ + 16 * i) * D_ + nc * 128) * 2), 0);
    };
    gl(0);
#pragma unroll 1
    for (int nc = 0; nc < 5; ++nc) {
        __syncthreads();
#pragma unroll
        for (int i = 0; i < 12; ++i) *(v4u*)(lds + wof + i * 4096) = rb[i];
        __syncthreads();
        if (nc + 1 < 5) gl(nc + 1);
        f32x4 ua[4][4];
#pragma unroll
        for (int mt = 0; mt < 4; ++mt)
#pragma unroll
            for (int nt = 0; nt < 4; ++nt) ua[mt][nt] = (f32x4){0.f, 0.f, 0.f, 0.f};
#pragma unroll
        for (int t = 0; t < 6; ++t) {
            bf16x8 wf[4];
#pragma unroll
            for (int nt = 0; nt < 4; ++nt) wf[nt] = trfragP<256>(lds, 32 * t, wn * 4 + nt, lane);
#pragma unroll
            for (int mt = 0; mt < 4; ++mt)
#pragma unroll
                for (int nt = 0; nt < 4; ++nt) ua[mt][nt] = mfma16(vf[mt][t], wf[nt], ua[mt][nt]);
        }
#pragma unroll
        for (int mt = 0; mt < 4; ++mt)
#pragma unroll
            for (int nt = 0; nt < 4; nt += 2) {
                int seg = rbk * 4 + wm * 2 + (mt >> 1), r = nc * 8 + wn * 4 + nt + (g & 1), jg = 2 * (mt & 1) + (g >> 1);
                unsigned off = (unsigned)OFF_UX + (unsigned)(((((seg * 32 + h) * 40 + r) * 4 + jg) * 16 + l15) * 16);
                st16c(rw, off, pair16(ua[mt][nt], ua[mt][nt + 1]));
            }
    }
}

__device__ __forceinline__ bool taskP(int s, int hq, char* ws, __amdgpu_buffer_rsrc_t rw) {
    int tid = threadIdx.x; asm volatile("" : "+v"(tid));
    const int lane = tid & 63, w = tid >> 6, g = lane >> 4, l15 = lane & 15;
    const float scale = 0.07216878364870322f;
    bool dead = false;
    v4u qf[2][6][2], kf[2][6][2], km[2][6];
#pragma unroll
    for (int hh = 0; hh < 2; ++hh) {
        const int h = hq * 8 + w * 2 + hh;
#pragma unroll
        for (int t = 0; t < 6; ++t)
#pragma unroll
            for (int tt = 0; tt < 2; ++tt) {
                unsigned o = (unsigned)(((s * 32 + 16 * tt + l15) * HE + h * E_ + 32 * t + 8 * g) * 2);
                qf[hh][t][tt] = ld16c(rw, (unsigned)OFF_Q + o); kf[hh][t][tt] = ld16c(rw, (unsigned)OFF_K + o);
            }
    }
    {
        unsigned spins = 0;
        const unsigned kb = (unsigned)OFF_KM + (unsigned)((((s * 32 + hq * 8 + w * 2) * 8 + (lane & 7)) * E_ + 8 * g) * 2);
        while (true) {
            asm volatile("" ::: "memory");
            bool ok = true;
#pragma unroll
            for (int hh = 0; hh < 2; ++hh)
#pragma unroll
                for (int t = 0; t < 6; ++t) km[hh][t] = ld16c(rw, kb + hh * (8 * E_ * 2) + t * 64);
#pragma unroll
            for (int hh = 0; hh < 2; ++hh)
#pragma unroll
                for (int t = 0; t < 6; ++t) ok &= okv(km[hh][t]);
            if (__all(ok)) break;
            if (++spins > kSpinCap) { dead = true; break; }
            __builtin_amdgcn_s_sleep(4);
        }
    }
#pragma unroll
    for (int hh = 0; hh < 2; ++hh) {
        const int h = hq * 8 + w * 2 + hh;
        f32x4 sc[3][2];
#pragma unroll
        for (int a = 0; a < 3; ++a) for (int b = 0; b < 2; ++b) sc[a][b] = (f32x4){0.f, 0.f, 0.f, 0.f};
#pragma unroll
        for (int t = 0; t < 6; ++t)
#pragma unroll
            for (int tt = 0; tt < 2; ++tt) {
                sc[0][tt] = mfma16(asf(km[hh][t]), asf(qf[hh][t][tt]), sc[0][tt]);
                sc[1][tt] = mfma16(asf(kf[hh][t][0]), asf(qf[hh][t][tt]), sc[1][tt]);
                sc[2][tt] = mfma16(asf(kf[hh][t][1]), asf(qf[hh][t][tt]), sc[2][tt]);
            }
#pragma unroll
        for (int tt = 0; tt < 2; ++tt) {
            const int tok = 16 * tt + l15; float mx = -3.0e38f;
#pragma unroll
            for (int a = 0; a < 3; ++a)
#pragma unroll
                for (int r = 0; r < 4; ++r) {
                    bool valid = (a == 0) ? (g < 2) : (16 * (a - 1) + 4 * g + r <= tok);
                    float v = valid ? rbf(rbf(sc[a][tt][r]) * scale) : -3.0e38f; sc[a][tt][r] = v; mx = fmaxf(mx, v);
                }
            mx = xmax32(xmax16(mx));
            float sum = 0.f;
#pragma unroll
            for (int a = 0; a < 3; ++a)
#pragma unroll
                for (int r = 0; r < 4; ++r) { float v = __expf(sc[a][tt][r] - mx); sc[a][tt][r] = v; sum += v; }
            sum = xsum32(xsum16(sum));
            const float inv = 1.f / sum;
            const unsigned pr = (unsigned)OFF_PT + (unsigned)(((s * 32 + tok) * 1280 + h * 40) * 2);
            v4u x = pair16(sc[1][tt] * inv, sc[2][tt] * inv), y = pair16(sc[0][tt] * inv, sc[0][tt] * inv);
            st16c(rw, pr + (1 + (g >> 1) + 2 * (g & 1)) * 16, x);
            if (g == 0) st16c(rw, pr, y);
        }
    }
    return dead;
}

constexpr int O_PITCH = 2576;
__device__ __forceinline__ bool taskO(char* lds, int s, int cq, const u16* __restrict__ x, char* ws, __amdgpu_buffer_rsrc_t rw, gu32* ctl) {
    int tid = threadIdx.x; asm volatile("" : "+v"(tid));
    const int lane = tid & 63, w = tid >> 6, g = lane >> 4, l15 = lane & 15;
    const int r0 = cq * 4 + w;
    bool dead = false;
    wait_cnt(ctl, CW_A + 32 * (s >> 2), 68);
    v4u ua[40];
    auto ldu = [&]() {
#pragma unroll
        for (int T = 0; T < 40; ++T) {
            int cg = 4 * T + g, h = cg / 5, jg = cg % 5;
            unsigned uo = (jg == 0) ? (unsigned)OFF_UM + (unsigned)(((s * 32 + h) * 40 + r0) * 256 + l15 * 16)
                                    : (unsigned)OFF_UX + (unsigned)(((((s * 32 + h) * 40 + r0) * 4 + jg - 1) * 16 + l15) * 16);
            ua[T] = ld16c(rw, uo);
        }
    };
    ldu();
    {
        unsigned spins = 0;
        while (true) {
            bool ok = true;
#pragma unroll
            for (int T = 0; T < 40; ++T) { bool um = ((4 * T + g) % 5 == 0); ok &= okv(ua[T]) || !um; }
            if (__all(ok)) break;
            if (spin_dead(spins, ctl)) { dead = true; break; }
            __builtin_amdgcn_s_sleep(16);
            asm volatile("" ::: "memory");
            ldu();
        }
    }
    wait_cnt(ctl, CW_P + 32 * s, 4);
    {
        const int row = tid >> 3, c0 = tid & 7;
        v4u pa[20];
#pragma unroll
        for (int j = 0; j < 20; ++j) pa[j] = ld16c(rw, (unsigned)OFF_PT + (unsigned)(((s * 32 + row) * 1280 + (c0 + 8 * j) * 8) * 2));
#pragma unroll
        for (int j = 0; j < 20; ++j) *(v4u*)(lds + row * O_PITCH + (c0 + 8 * j) * 16) = pa[j];
    }
    __syncthreads();
    f32x4 acc[2][2] = {{{0.f, 0.f, 0.f, 0.f}, {0.f, 0.f, 0.f, 0.f}}, {{0.f, 0.f, 0.f, 0.f}, {0.f, 0.f, 0.f, 0.f}}};
#pragma unroll
    for (int T = 0; T < 40; ++T)
#pragma unroll
        for (int tt = 0; tt < 2; ++tt) {
            bf16x8 pb = *(const bf16x8*)(lds + (16 * tt + l15) * O_PITCH + (4 * T + g) * 16);
            acc[tt][T & 1] = mfma16(asf(ua[T]), pb, acc[tt][T & 1]);
        }
    const float* mod = (const float*)(ws + OFF_MOD);
#pragma unroll
    for (int tt = 0; tt < 2; ++tt) {
        int tok = s * 32 + 16 * tt + l15, c = 16 * r0 + 4 * g;
        v2u xv = *(const v2u*)(x + (size_t)tok * D_ + c);
        float xs[4] = {bflo(xv.x), bfhi(xv.x), bflo(xv.y), bfhi(xv.y)}; f32x4 o; f32x4 av = acc[tt][0] + acc[tt][1];
#pragma unroll
        for (int j = 0; j < 4; ++j) o[j] = xs[j] + rbf(mod[2 * 640 + c + j] * rbf(av[j]));
        v4u pv = pair16(o, o);
        if ((g & 1) == 0) st16c(rw, (unsigned)OFF_X1 + (unsigned)((tok * D_ + 16 * r0 + 8 * (g >> 1)) * 2), pv);
    }
    return dead;
}

constexpr int F1_APITCH = 1296;
constexpr int F1_BOFF = 32 * F1_APITCH;
constexpr int F1_ROFF = F1_BOFF;
__device__ __forceinline__ void taskF1(char* lds, int s, int ct, const u16* __restrict__ w1, const u16* __restrict__ w3, char* ws, __amdgpu_buffer_rsrc_t rw, gu32* ctl) {
    int tid = threadIdx.x; asm volatile("" : "+v"(tid));
    const int lane = tid & 63, w = tid >> 6, g = lane >> 4, l15 = lane & 15;
    const __amdgpu_buffer_rsrc_t r1 = mkrs(w1, D_ * F_ * 2), r3 = mkrs(w3, D_ * F_ * 2);
    v4u bw[5][6];
    unsigned vo[3]; int lo[3];
#pragma unroll
    for (int i = 0; i < 3; ++i) { int idx = lane + 64 * i, row = idx / 6, c = idx % 6; vo[i] = (unsigned)((row * F_ + c * 8) * 2);
        lo[i] = row * 192 + c * 16; }
#pragma unroll
    for (int j = 0; j < 5; ++j)
#pragma unroll
        for (int i = 0; i < 3; ++i) {
            unsigned so = (unsigned)(((5 * w + j) * 32 * F_ + ct * 48) * 2);
            bw[j][i] = __builtin_amdgcn_raw_buffer_load_b128(r1, vo[i], so, 0); bw[j][3 + i] = __builtin_amdgcn_raw_buffer_load_b128(r3, vo[i], so, 0);
        }
    wait_cnt(ctl, CW_X + 32 * s, 10);
    {
        const int row = tid >> 3, c0 = tid & 7;
        v4u xa[10];
#pragma unroll
        for (int j = 0; j < 10; ++j) xa[j] = ld16c(rw, (unsigned)OFF_X1 + (unsigned)(((s * 32 + row) * D_ + (c0 + 8 * j) * 8) * 2));
        float sum = 0.f;
#pragma unroll
        for (int j = 0; j < 10; ++j) sum += bflo(xa[j].x) + bfhi(xa[j].x) + bflo(xa[j].y) + bfhi(xa[j].y) + bflo(xa[j].z) + bfhi(xa[j].z) + bflo(xa[j].w) + bfhi(xa[j].w);
        sum += __shfl_xor(sum, 1); sum += __shfl_xor(sum, 2); sum += __shfl_xor(sum, 4);
        const float mean = sum * (1.f / D_); float q = 0.f;
#pragma unroll
        for (int j = 0; j < 10; ++j) { float d;
            d = bflo(xa[j].x) - mean; q += d * d; d = bfhi(xa[j].x) - mean; q += d * d; d = bflo(xa[j].y) - mean; q += d * d; d = bfhi(xa[j].y) - mean; q += d * d;
            d = bflo(xa[j].z) - mean; q += d * d; d = bfhi(xa[j].z) - mean; q += d * d; d = bflo(xa[j].w) - mean; q += d * d; d = bfhi(xa[j].w) - mean; q += d * d; }
        q += __shfl_xor(q, 1); q += __shfl_xor(q, 2); q += __shfl_xor(q, 4);
        const float rstd = 1.f / sqrtf(q * (1.f / D_) + 1e-5f);
        const float* mod = (const float*)(ws + OFF_MOD);
#pragma unroll
        for (int j = 0; j < 10; ++j) {
            const int c = (c0 + 8 * j) * 8;
            f32x4 m0 = *(const f32x4*)(mod + 4 * 640 + c), m1 = *(const f32x4*)(mod + 4 * 640 + c + 4), s0 = *(const f32x4*)(mod + 3 * 640 + c), s1 = *(const f32x4*)(mod + 3 * 640 + c + 4);
            v4u u = xa[j], o;
            o.x = pk2((bflo(u.x) - mean) * rstd * (1.f + m0[0]) + s0[0], (bfhi(u.x) - mean) * rstd * (1.f + m0[1]) + s0[1]);
            o.y = pk2((bflo(u.y) - mean) * rstd * (1.f + m0[2]) + s0[2], (bfhi(u.y) - mean) * rstd * (1.f + m0[3]) + s0[3]);
            o.z = pk2((bflo(u.z) - mean) * rstd * (1.f + m1[0]) + s1[0], (bfhi(u.z) - mean) * rstd * (1.f + m1[1]) + s1[1]);
            o.w = pk2((bflo(u.w) - mean) * rstd * (1.f + m1[2]) + s1[2], (bfhi(u.w) - mean) * rstd * (1.f + m1[3]) + s1[3]);
            *(v4u*)(lds + row * F1_APITCH + (c0 + 8 * j) * 16) = o;
        }
    }
    __syncthreads();
    f32x4 acc[2][6];
#pragma unroll
    for (int a = 0; a < 2; ++a) for (int b = 0; b < 6; ++b) acc[a][b] = (f32x4){0.f, 0.f, 0.f, 0.f};
    char* bb = lds + F1_BOFF + w * 12288;
    int so2[6];
#pragma unroll
    for (int i = 0; i < 3; ++i) { int idx = lane + 64 * i, row = idx / 6, c = idx % 6, f = (row >> 3) & 1;
        so2[i] = row * 192 + (((c >> 1) ^ f) << 5) + ((c & 1) << 4); so2[3 + i] = row * 192 + ((((6 + c) >> 1) ^ f) << 5) + ((c & 1) << 4); }
    const int q4 = (lane & 15) >> 2, p4 = lane & 3;
#pragma unroll
    for (int j = 0; j < 5; ++j) {
        char* buf = bb + (j & 1) * 6144;
#pragma unroll
        for (int i = 0; i < 6; ++i) *(v4u*)(buf + so2[i]) = bw[j][i];
        bf16x8 af[2], bfr[6];
#pragma unroll
        for (int mt = 0; mt < 2; ++mt) af[mt] = *(const bf16x8*)(lds + (16 * mt + l15) * F1_APITCH + (4 * (5 * w + j) + g) * 16);
#pragma unroll
        for (int nt = 0; nt < 6; ++nt) { const int a = (8 * g + q4) * 192 + ((nt ^ (g & 1)) << 5) + 8 * p4; bfr[nt] = cat8(trread(buf, a), trread(buf, a + 4 * 192)); }
#pragma unroll
        for (int mt = 0; mt < 2; ++mt)
#pragma unroll
            for (int nt = 0; nt < 6; ++nt) acc[mt][nt] = mfma16(bfr[nt], af[mt], acc[mt][nt]);
    }
    __syncthreads();
    f32x4* red = (f32x4*)(lds + F1_ROFF);
#pragma unroll
    for (int mt = 0; mt < 2; ++mt)
#pragma unroll
        for (int nt = 0; nt < 6; ++nt) red[(w * 12 + mt * 6 + nt) * 64 + lane] = acc[mt][nt];
    __syncthreads();
    {
        const int mt = w & 1; f32x4 o[2];
#pragma unroll
        for (int n = 0; n < 2; ++n) {
            const int nt = (w < 2) ? n : 2;
            f32x4 ga = (red[(0 * 12 + mt * 6 + nt) * 64 + lane] + red[(1 * 12 + mt * 6 + nt) * 64 + lane]) + (red[(2 * 12 + mt * 6 + nt) * 64 + lane] + red[(3 * 12 + mt * 6 + nt) * 64 + lane]);
            f32x4 up = (red[(0 * 12 + mt * 6 + 3 + nt) * 64 + lane] + red[(1 * 12 + mt * 6 + 3 + nt) * 64 + lane]) + (red[(2 * 12 + mt * 6 + 3 + nt) * 64 + lane] + red[(3 * 12 + mt * 6 + 3 + nt) * 64 + lane]);
#pragma unroll
            for (int j = 0; j < 4; ++j) { float a = rbf(ga[j]), u = rbf(up[j]); float sl = rbf(a / (1.f + __expf(-a))); o[n][j] = sl * u; }
        }
        const int tok = s * 32 + 16 * mt + l15;
        v4u v = pair16(o[0], o[1]);
        if (w < 2) st16c(rw, (unsigned)OFF_ACT + (unsigned)((tok * F_ + ct * 48 + (g & 1) * 16 + 8 * (g >> 1)) * 2), v);
        else if ((g & 1) == 0) st16c(rw, (unsigned)OFF_ACT + (unsigned)((tok * F_ + ct * 48 + 32 + 8 * (g >> 1)) * 2), v);
    }
}

constexpr int F2_APITCH = 3088;
constexpr int F2_BOFF = 32 * F2_APITCH;
__device__ __forceinline__ void taskF2(char* lds, int s, int ct, const u16* __restrict__ w2, char* ws, u16* __restrict__ out, __amdgpu_buffer_rsrc_t rw, gu32* ctl) {
    int tid = threadIdx.x; asm volatile("" : "+v"(tid));
    const int lane = tid & 63, w = tid >> 6, g = lane >> 4, l15 = lane & 15;
    const __amdgpu_buffer_rsrc_t r2 = mkrs(w2, F_ * D_ * 2);
    v4u bw[12][4];
#pragma unroll
    for (int j = 0; j < 12; ++j)
#pragma unroll
        for (int i = 0; i < 4; ++i)
            bw[j][i] = __builtin_amdgcn_raw_buffer_load_b128(r2, (unsigned)((((lane >> 3) + 8 * i) * D_ + (lane & 7) * 8) * 2), (unsigned)(((12 * w + j) * 32 * D_ + ct * 64) * 2), 0);
    wait_cnt(ctl, CW_F + 32 * s, 32);
    {
        const int row = tid >> 3, c0 = tid & 7;
#pragma unroll
        for (int j0 = 0; j0 < 24; j0 += 12) {
            v4u xa[12];
#pragma unroll
            for (int j = 0; j < 12; ++j) xa[j] = ld16c(rw, (unsigned)OFF_ACT + (unsigned)(((s * 32 + row) * F_ + (c0 + 8 * (j0 + j)) * 8) * 2));
#pragma unroll
            for (int j = 0; j < 12; ++j) *(v4u*)(lds + row * F2_APITCH + (c0 + 8 * (j0 + j)) * 16) = xa[j];
        }
    }
    __syncthreads();
    f32x4 acc[2][4];
#pragma unroll
    for (int a = 0; a < 2; ++a) for (int b = 0; b < 4; ++b) acc[a][b] = (f32x4){0.f, 0.f, 0.f, 0.f};
    char* bb = lds + F2_BOFF + w * 8192;
    int so2[4];
#pragma unroll
    for (int i = 0; i < 4; ++i) { int row = (lane >> 3) + 8 * i, c = lane & 7, f = ((row >> 1) & 1) | (((row >> 3) & 1) << 1); so2[i] = row * 128 + (((c >> 1) ^ f) << 5) + ((c & 1) << 4); }
    const int q4 = (lane & 15) >> 2, p4 = lane & 3, ff = ((q4 >> 1) & 1) | ((g & 1) << 1);
#pragma unroll
    for (int j = 0; j < 12; ++j) {
        char* buf = bb + (j & 1) * 4096;
#pragma unroll
        for (int i = 0; i < 4; ++i) *(v4u*)(buf + so2[i]) = bw[j][i];
        bf16x8 af[2], bfr[4];
#pragma unroll
        for (int mt = 0; mt < 2; ++mt) af[mt] = *(const bf16x8*)(lds + (16 * mt + l15) * F2_APITCH + (4 * (12 * w + j) + g) * 16);
#pragma unroll
        for (int nt = 0; nt < 4; ++nt) { const int a = (8 * g + q4) * 128 + ((nt ^ ff) << 5) + 8 * p4; bfr[nt] = cat8(trread(buf, a), trread(buf, a + 4 * 128)); }
#pragma unroll
        for (int mt = 0; mt < 2; ++mt)
#pragma unroll
            for (int nt = 0; nt < 4; ++nt) acc[mt][nt] = mfma16(bfr[nt], af[mt], acc[mt][nt]);
    }
    __syncthreads();
    f32x4* red = (f32x4*)lds;
#pragma unroll
    for (int mt = 0; mt < 2; ++mt)
#pragma unroll
        for (int nt = 0; nt < 4; ++nt) red[(w * 8 + mt * 4 + nt) * 64 + lane] = acc[mt][nt];
    __syncthreads();
    {
        const float* mod = (const float*)(ws + OFF_MOD);
        const int mt = w & 1, n0 = (w >> 1) * 2; f32x4 o[2];
        const int tok = s * 32 + 16 * mt + l15;
#pragma unroll
        for (int n = 0; n < 2; ++n) {
            const int T = mt * 4 + n0 + n;
            f32x4 v = (red[(0 * 8 + T) * 64 + lane] + red[(1 * 8 + T) * 64 + lane]) + (red[(2 * 8 + T) * 64 + lane] + red[(3 * 8 + T) * 64 + lane]);
            const int c = ct * 64 + (n0 + n) * 16 + 4 * g;
            v2u xv = ld8c(rw, (unsigned)OFF_X1 + (unsigned)((tok * D_ + c) * 2));
            float xs[4] = {bflo(xv.x), bfhi(xv.x), bflo(xv.y), bfhi(xv.y)};
#pragma unroll
            for (int j = 0; j < 4; ++j) o[n][j] = xs[j] + rbf(mod[5 * 640 + c + j] * rbf(v[j]));
        }
        *(v4u*)(out + (size_t)tok * D_ + ct * 64 + (n0 + (g & 1)) * 16 + 8 * (g >> 1)) = pair16(o[0], o[1]);
    }
}

constexpr int B2_ST = 2 * GBUF;
__device__ __forceinline__ void taskF1B(char* lds, int rbk, int ct, const u16* __restrict__ w1, const u16* __restrict__ w3, char* ws, __amdgpu_buffer_rsrc_t rw) {
    int tid = threadIdx.x; asm volatile("" : "+v"(tid));
    const int lane = tid & 63, w = tid >> 6, wm = w >> 1, wn = w & 1, g = lane >> 4, l15 = lane & 15;
    float2* stats = (float2*)(lds + B2_ST);
    { const float* mod = (const float*)(ws + OFF_MOD); float* ml = (float*)(lds + B2_ST + 1024);
      for (int i = tid; i < 640; i += 256) { ml[i] = 1.f + mod[4 * 640 + i]; ml[640 + i] = mod[3 * 640 + i]; } }
    const unsigned xb = (unsigned)OFF_X1 + (unsigned)(rbk * 128 * D_ * 2);
#pragma unroll 1
    for (int rr = 0; rr < 4; ++rr) {
        const int row = rr * 32 + (tid >> 3), c0 = tid & 7;
        v4u xa[10];
#pragma unroll
        for (int j = 0; j < 10; ++j) xa[j] = ld16c(rw, xb + (unsigned)((row * D_ + (c0 + 8 * j) * 8) * 2));
        float sum = 0.f;
#pragma unroll
        for (int j = 0; j < 10; ++j) sum += bflo(xa[j].x) + bfhi(xa[j].x) + bflo(xa[j].y) + bfhi(xa[j].y) + bflo(xa[j].z) + bfhi(xa[j].z) + bflo(xa[j].w) + bfhi(xa[j].w);
        sum += __shfl_xor(sum, 1); sum += __shfl_xor(sum, 2); sum += __shfl_xor(sum, 4);
        const float mean = sum * (1.f / D_); float q = 0.f;
#pragma unroll
        for (int j = 0; j < 10; ++j) { float d;
            d = bflo(xa[j].x) - mean; q += d * d; d = bfhi(xa[j].x) - mean; q += d * d; d = bflo(xa[j].y) - mean; q += d * d; d = bfhi(xa[j].y) - mean; q += d * d;
            d = bflo(xa[j].z) - mean; q += d * d; d = bfhi(xa[j].z) - mean; q += d * d; d = bflo(xa[j].w) - mean; q += d * d; d = bfhi(xa[j].w) - mean; q += d * d; }
        q += __shfl_xor(q, 1); q += __shfl_xor(q, 2); q += __shfl_xor(q, 4);
        if (c0 == 0) stats[row] = make_float2(mean, 1.f / sqrtf(q * (1.f / D_) + 1e-5f));
    }
    __syncthreads();
    const float* modl = (const float*)(lds + B2_ST + 1024);
    f32x4 acc[4][6];
    const int bt = tid < 192 ? tid : 0, brr = bt / 12, bc = bt % 12;
    const unsigned vb = (unsigned)((brr * F_ + (bc / 6) * 48 + (bc % 6) * 8) * 2);
    gemm4<10, PFD, 16>(lds, rw, (unsigned)(((tid >> 3) * D_ + (tid & 7) * 8) * 2), xb, D_ * 2,
          mkrs(w1, D_ * F_ * 2), mkrs(w3, D_ * F_ * 2), vb, vb, (unsigned)(ct * 96 * 2), F_ * 2, (bc / 6) * 12 + bc % 6, (bc / 6) * 12 + 6 + bc % 6,
          [&](int kc, int r, int ch, v4u u) {
              float2 st = stats[r]; const int c = kc * 64 + ch * 8;
              f32x4 m0 = *(const f32x4*)(modl + c), m1 = *(const f32x4*)(modl + c + 4), s0 = *(const f32x4*)(modl + 640 + c), s1 = *(const f32x4*)(modl + 640 + c + 4);
              v4u o;
              o.x = pk2((bflo(u.x) - st.x) * st.y * m0[0] + s0[0], (bfhi(u.x) - st.x) * st.y * m0[1] + s0[1]);
              o.y = pk2((bflo(u.y) - st.x) * st.y * m0[2] + s0[2], (bfhi(u.y) - st.x) * st.y * m0[3] + s0[3]);
              o.z = pk2((bflo(u.z) - st.x) * st.y * m1[0] + s1[0], (bfhi(u.z) - st.x) * st.y * m1[1] + s1[1]);
              o.w = pk2((bflo(u.w) - st.x) * st.y * m1[2] + s1[2], (bfhi(u.w) - st.x) * st.y * m1[3] + s1[3]);
              return o; }, acc);
#pragma unroll
    for (int mt = 0; mt < 4; ++mt) {
        f32x4 o[4];
#pragma unroll
        for (int nt = 0; nt < 3; ++nt)
#pragma unroll
            for (int j = 0; j < 4; ++j) { float ga = rbf(acc[mt][nt][j]), up = rbf(acc[mt][nt + 3][j]); float sl = rbf(ga / (1.f + __expf(-ga))); o[nt][j] = sl * up; }
        int tok = rbk * 128 + wm * 64 + mt * 16 + l15;
        int f0 = ct * 96 + wn * 48 + (g & 1) * 16 + 8 * (g >> 1);
        st16c(rw, (unsigned)OFF_ACT + (unsigned)((tok * F_ + f0) * 2), pair16(o[0], o[1]));
        v4u y = pair16(o[2], o[2]);
        int f1 = ct * 96 + wn * 48 + 32 + 8 * (g >> 1);
        if ((g & 1) == 0) st16c(rw, (unsigned)OFF_ACT + (unsigned)((tok * F_ + f1) * 2), y);
    }
}
__device__ __forceinline__ void taskF2B(char* lds, int rbk, int ct, const u16* __restrict__ w2, char* ws, u16* __restrict__ out, __amdgpu_buffer_rsrc_t rw) {
    int tid = threadIdx.x; asm volatile("" : "+v"(tid));
    const int lane = tid & 63, w = tid >> 6, wm = w >> 1, wn = w & 1, g = lane >> 4, l15 = lane & 15;
    f32x4 acc[4][6];
    const unsigned ab = (unsigned)OFF_ACT + (unsigned)(rbk * 128 * F_ * 2);
    const int bt = tid < 192 ? tid : 0, brr = bt / 12, bc = bt % 12;
    const __amdgpu_buffer_rsrc_t rW = mkrs(w2, F_ * D_ * 2);
    int c0 = ct * 192 + bc * 8, c1 = ct * 192 + (12 + bc) * 8; if (c0 >= D_) c0 = 0; if (c1 >= D_) c1 = 0;
    gemm4<24, PFD2, 16>(lds, rw, (unsigned)(((tid >> 3) * F_ + (tid & 7) * 8) * 2), ab, F_ * 2,
          rW, rW, (unsigned)((brr * D_ + c0) * 2), (unsigned)((brr * D_ + c1) * 2), 0u, D_ * 2, bc, 12 + bc, [](int, int, int, v4u v) { return v; }, acc);
    const float* mod = (const float*)(ws + OFF_MOD);
#pragma unroll
    for (int mt = 0; mt < 4; ++mt)
#pragma unroll
        for (int nt = 0; nt < 6; ++nt) {
            int tok = rbk * 128 + wm * 64 + mt * 16 + l15, c = ct * 192 + wn * 96 + nt * 16 + 4 * g;
            if (c < D_) {
                v2u xv = ld8c(rw, (unsigned)OFF_X1 + (unsigned)((tok * D_ + c) * 2));
                float xs[4] = {bflo(xv.x), bfhi(xv.x), bflo(xv.y), bfhi(xv.y)}; float o[4];
#pragma unroll
                for (int j = 0; j < 4; ++j) o[j] = xs[j] + rbf(mod[5 * 640 + c + j] * rbf(acc[mt][nt][j]));
                *(v2u*)(out + (size_t)tok * D_ + c) = (v2u){pk2(o[0], o[1]), pk2(o[2], o[3])};
            }
        }
}

#define REDMODE 0
__device__ __forceinline__ void taskFill(int s, __amdgpu_buffer_rsrc_t rw) {
    int tid = threadIdx.x; asm volatile("" : "+v"(tid));
    const v4u sv = {SENT, SENT, SENT, SENT};
    for (int i = tid; i < 640; i += 256) st16c(rw, (unsigned)OFF_MEM + (unsigned)((s + 1) * 10240 + i * 16), sv);
    for (int i = tid; i < 1280; i += 256) st16c(rw, (unsigned)OFF_P + (unsigned)(s * 20480 + i * 16), sv);
    for (int i = tid; i < 20480; i += 256) st16c(rw, (unsigned)OFF_UM + (unsigned)(s * 327680 + i * 16), sv);
    for (int i = tid; i < 6144; i += 256) st16c(rw, (unsigned)OFF_KM + (unsigned)(s * 98304 + i * 16), sv);
}
__device__ __forceinline__ void taskLN(int sg, const u16* __restrict__ x, char* ws, __amdgpu_buffer_rsrc_t rw) {
    int tid = threadIdx.x; asm volatile("" : "+v"(tid));
    const float* mod = (const float*)(ws + OFF_MOD);
    {
        const int row = sg * 32 + (tid >> 3), c0 = tid & 7;
        v4u xa[10];
#pragma unroll
        for (int j = 0; j < 10; ++j) xa[j] = *(const v4u*)(x + (size_t)row * D_ + (c0 + 8 * j) * 8);
        float sum = 0.f;
#pragma unroll
        for (int j = 0; j < 10; ++j) sum += bflo(xa[j].x) + bfhi(xa[j].x) + bflo(xa[j].y) + bfhi(xa[j].y) + bflo(xa[j].z) + bfhi(xa[j].z) + bflo(xa[j].w) + bfhi(xa[j].w);
        sum += __shfl_xor(sum, 1); sum += __shfl_xor(sum, 2); sum += __shfl_xor(sum, 4);
        const float mean = sum * (1.f / D_); float q = 0.f;
#pragma unroll
        for (int j = 0; j < 10; ++j) { float d;
            d = bflo(xa[j].x) - mean; q += d * d; d = bfhi(xa[j].x) - mean; q += d * d; d = bflo(xa[j].y) - mean; q += d * d; d = bfhi(xa[j].y) - mean; q += d * d;
            d = bflo(xa[j].z) - mean; q += d * d; d = bfhi(xa[j].z) - mean; q += d * d; d = bflo(xa[j].w) - mean; q += d * d; d = bfhi(xa[j].w) - mean; q += d * d; }
        q += __shfl_xor(q, 1); q += __shfl_xor(q, 2); q += __shfl_xor(q, 4);
        const float rstd = 1.f / sqrtf(q * (1.f / D_) + 1e-5f);
#pragma unroll
        for (int j = 0; j < 10; ++j) {
            const int c = (c0 + 8 * j) * 8;
            f32x4 m0 = *(const f32x4*)(mod + 640 + c), m1 = *(const f32x4*)(mod + 640 + c + 4), s0 = *(const f32x4*)(mod + c), s1 = *(const f32x4*)(mod + c + 4);
            v4u u = xa[j], o;
            o.x = pk2((bflo(u.x) - mean) * rstd * (1.f + m0[0]) + s0[0], (bfhi(u.x) - mean) * rstd * (1.f + m0[1]) + s0[1]);
            o.y = pk2((bflo(u.y) - mean) * rstd * (1.f + m0[2]) + s0[2], (bfhi(u.y) - mean) * rstd * (1.f + m0[3]) + s0[3]);
            o.z = pk2((bflo(u.z) - mean) * rstd * (1.f + m1[0]) + s1[0], (bfhi(u.z) - mean) * rstd * (1.f + m1[1]) + s1[1]);
            o.w = pk2((bflo(u.w) - mean) * rstd * (1.f + m1[2]) + s1[2], (bfhi(u.w) - mean) * rstd * (1.f + m1[3]) + s1[3]);
            st16c(rw, (unsigned)OFF_H + (unsigned)((row * D_ + c) * 2), o);
        }
    }
}

constexpr int C_RES = 0;
constexpr int C_MEM = 122880;
constexpr int C_SP  = 133120;
constexpr int C_FLAG = 145408;
constexpr int C_LDS = 145408 + 64;

__device__ __forceinline__ int lds_flag_get(char* lds) { return *(volatile LAS int*)(LAS char*)(lds + C_FLAG); }
__device__ __forceinline__ void lds_flag_set(char* lds, int v) { *(volatile LAS int*)(LAS char*)(lds + C_FLAG) = v; }
__device__ __forceinline__ bool poll_mem(__amdgpu_buffer_rsrc_t rs, unsigned goff, char* ldst, int tid, gu32* ctl) {
    v4u v[3]; unsigned spins = 0; bool dead = false;
    while (true) {
        asm volatile("" ::: "memory");
        v[0] = ld16c(rs, goff + tid * 16); v[1] = ld16c(rs, goff + (tid + 256) * 16);
        if (tid < 128) v[2] = ld16c(rs, goff + (tid + 512) * 16); else v[2] = v[1];
        bool ok = okv(v[0]) && okv(v[1]) && okv(v[2]);
        if (ok) break;
        if (spin_dead(spins, ctl)) { dead = true; break; }
    }
    *(v4u*)(ldst + tid * 16) = v[0]; *(v4u*)(ldst + (tid + 256) * 16) = v[1];
    if (tid < 128) *(v4u*)(ldst + (tid + 512) * 16) = v[2];
    return dead;
}

template <int NR, int BT> __device__ __forceinline__ void stage192(char* ldst, const u16* __restrict__ gsrc, int tid) {
    if (tid < 192) {
        const int r0 = tid / 24, ch = tid % 24;
        const u16* gp = gsrc + (size_t)r0 * HE + ch * 8;
        const int o0 = r0 * 384 + (((ch >> 1) ^ ((r0 >> 1) & 1)) << 5) + ((ch & 1) << 4);
        const int o1 = r0 * 384 + (((ch >> 1) ^ (((r0 >> 1) & 1) | 2)) << 5) + ((ch & 1) << 4);
#pragma unroll 1
        for (int i0 = 0; i0 < NR / 8; i0 += BT) {
            v4u r[BT];
#pragma unroll
            for (int i = 0; i < BT; ++i) r[i] = *(const v4u*)(gp + (size_t)(i0 + i) * 8 * HE);
#pragma unroll
            for (int i = 0; i < BT; ++i) *(v4u*)(ldst + ((i & 1) ? o1 : o0) + (i0 + i) * 3072) = r[i];
        }
    }
}
__device__ __forceinline__ void stage640(char* ldst, const u16* __restrict__ gsrc, int tid) {
    const int r0 = tid >> 4, c0 = tid & 15;
    const int f = (r0 & 3) | (((r0 >> 3) & 1) << 2);
    const u16* gp = gsrc + (size_t)r0 * D_ + c0 * 8;
    const int o = r0 * 1280 + (((c0 >> 1) ^ f) << 5) + ((c0 & 1) << 4);
#pragma unroll 1
    for (int i = 0; i < 6; ++i) {
        v4u r[5];
#pragma unroll
        for (int j = 0; j < 5; ++j) r[j] = *(const v4u*)(gp + (size_t)i * 16 * D_ + j * 128);
#pragma unroll
        for (int j = 0; j < 5; ++j) *(v4u*)(ldst + o + i * 16 * 1280 + j * 256) = r[j];
    }
}

__device__ __forceinline__ void chain_qk(char* lds, int h, const u16* __restrict__ Wq, const u16* __restrict__ Wk, char* ws, __amdgpu_buffer_rsrc_t rs, __amdgpu_buffer_rsrc_t rw) {
    gu32* ctl = (gu32*)(ws + OFF_CTL);
    const int tid = threadIdx.x, lane = tid & 63, w = tid >> 6, g = lane >> 4, l15 = lane & 15;
    const int et0 = 3 * w;
    bf16x8 wq[3][15], wk[3][15];
#pragma unroll
    for (int c = 0; c < 3; ++c) {
        __syncthreads();
        stage192<160, 10>(lds, Wq + (size_t)(c * 160) * HE + h * E_, tid); stage192<160, 10>(lds + 61440, Wk + (size_t)(c * 160) * HE + h * E_, tid);
        __syncthreads();
#pragma unroll
        for (int k5 = 0; k5 < 5; ++k5)
#pragma unroll
            for (int j = 0; j < 3; ++j) { wq[j][c * 5 + k5] = trfrag192(lds, 32 * k5, et0 + j, lane); wk[j][c * 5 + k5] = trfrag192(lds + 61440, 32 * k5, et0 + j, lane); }
    }
    __syncthreads();
    stage192<160, 4>(lds + C_RES, Wq + (size_t)480 * HE + h * E_, tid); stage192<160, 4>(lds + C_RES + 61440, Wk + (size_t)480 * HE + h * E_, tid);
    const unsigned kg0 = (unsigned)OFF_K + (unsigned)((l15 * HE + h * E_ + 48 * w + 4 * g) * 2);
    v2u kx[2][3];
    wait_cnt(ctl, CW_A, 68);
#pragma unroll
    for (int kt = 0; kt < 2; ++kt)
#pragma unroll
        for (int j = 0; j < 3; ++j) kx[kt][j] = ld8c(rw, kg0 + (unsigned)((16 * kt) * HE * 2 + 32 * j));
    if (tid == 0) lds_flag_set(lds, 0);
    __syncthreads();
    bf16x8 la[6];
#pragma unroll
    for (int j = 0; j < 3; ++j) { la[2 * j] = trfrag192(lds + C_RES, 0, et0 + j, lane); la[2 * j + 1] = trfrag192(lds + C_RES + 61440, 0, et0 + j, lane); }
    const float scale = 0.07216878364870322f;
#pragma unroll 1
    for (int s = 0; s < NSEG; ++s) {
        ST(h, s, 0);
        poll_mem(rs, s * 10240, lds + C_MEM, tid, ctl);
        bar_lds();
        ST(h, s, 1);
        f32x4 aq[3], ak[3];
#pragma unroll
        for (int j = 0; j < 3; ++j) { aq[j] = (f32x4){0.f, 0.f, 0.f, 0.f}; ak[j] = (f32x4){0.f, 0.f, 0.f, 0.f}; }
        {
            const char* mb = lds + C_MEM + (g * 8 + (lane & 7)) * 16;
            bf16x8 b[20];
            b[0] = *(const bf16x8*)(mb + 0 * 512); b[1] = *(const bf16x8*)(mb + 1 * 512);
#pragma unroll
            for (int p = 0; p < 20; ++p) {
                const int ks = ((p & 3) == 3) ? 15 + (p >> 2) : 3 * (p >> 2) + (p & 3);
                if (p + 2 < 20) { const int k2 = (((p + 2) & 3) == 3) ? 15 + ((p + 2) >> 2) : 3 * ((p + 2) >> 2) + ((p + 2) & 3); b[p + 2] = *(const bf16x8*)(mb + k2 * 512); }
#pragma unroll
                for (int j = 0; j < 3; ++j) {
                    bf16x8 a0 = ((p & 3) != 3) ? wq[j][ks < 15 ? ks : 0] : la[2 * j];
                    aq[j] = mfma16(a0, b[p], aq[j]);
                    bf16x8 a1 = ((p & 3) != 3) ? wk[j][ks < 15 ? ks : 0] : la[2 * j + 1];
                    ak[j] = mfma16(a1, b[p], ak[j]);
                }
                SB();
                if ((p & 3) == 3) {
#pragma unroll
                    for (int j = 0; j < 3; ++j) { la[2 * j] = trfrag192(lds + C_RES, 32 * (((p >> 2) + 1) % 5), et0 + j, lane); la[2 * j + 1] = trfrag192(lds + C_RES + 61440, 32 * (((p >> 2) + 1) % 5), et0 + j, lane); }
                    SB();
                }
            }
        }
        v2u qp[3], kp[3];
#pragma unroll
        for (int j = 0; j < 3; ++j) { qp[j] = (v2u){pk2(aq[j][0], aq[j][1]), pk2(aq[j][2], aq[j][3])}; kp[j] = (v2u){pk2(ak[j][0], ak[j][1]), pk2(ak[j][2], ak[j][3])}; }
        if (s + 1 < NSEG) {
            bf16x8 q0 = __builtin_bit_cast(bf16x8, (v4u){qp[0].x, qp[0].y, qp[1].x, qp[1].y}), q1 = __builtin_bit_cast(bf16x8, (v4u){qp[2].x, qp[2].y, 0u, 0u});
            bf16x8 k0 = __builtin_bit_cast(bf16x8, (v4u){kp[0].x, kp[0].y, kp[1].x, kp[1].y}), k1 = __builtin_bit_cast(bf16x8, (v4u){kp[2].x, kp[2].y, 0u, 0u});
            f32x4 z = {0.f, 0.f, 0.f, 0.f};
            f32x4 s0 = mfma16(k1, q1, mfma16(k0, q0, z));
            f32x4 sx[2];
#pragma unroll
            for (int kt = 0; kt < 2; ++kt) {
                bf16x8 x0 = __builtin_bit_cast(bf16x8, (v4u){kx[kt][0].x, kx[kt][0].y, kx[kt][1].x, kx[kt][1].y}), x1 = __builtin_bit_cast(bf16x8, (v4u){kx[kt][2].x, kx[kt][2].y, 0u, 0u});
                sx[kt] = mfma16(x1, q1, mfma16(x0, q0, z));
            }
            f32x4* sp = (f32x4*)(lds + C_SP) + w * 192 + lane;
            sp[0] = s0; sp[64] = sx[0]; sp[128] = sx[1];
        }
        bar_lds();
        ST(h, s, 2);
        if (w == 0 && s + 1 < NSEG) {
            const f32x4* sp = (const f32x4*)(lds + C_SP) + (l15 >> 3) * 384 + lane;
            f32x4 sc[3], p0[3], p1[3];
#pragma unroll
            for (int a = 0; a < 3; ++a) { p0[a] = sp[a * 64]; p1[a] = sp[192 + a * 64]; }
#pragma unroll
            for (int a = 0; a < 3; ++a) {
                f32x4 t = p0[a] + p1[a];
#pragma unroll
                for (int r = 0; r < 4; ++r) sc[a][r] = t[r] + __uint_as_float(__builtin_amdgcn_mov_dpp(__float_as_uint(t[r]), 0x128, 0xf, 0xf, true));
            }
            const float sl2 = scale * 1.4426950408889634f;
            float mx = -3.0e38f;
#pragma unroll
            for (int r = 0; r < 4; ++r) { float t0 = sc[0][r] * sl2; sc[0][r] = (g < 2) ? t0 : -3.0e38f; sc[1][r] *= sl2; sc[2][r] *= sl2;
                mx = fmaxf(mx, fmaxf(sc[0][r], fmaxf(sc[1][r], sc[2][r]))); }
            mx = xmax32(xmax16(mx));
            float sum = 0.f;
#pragma unroll
            for (int r = 0; r < 4; ++r) { sc[0][r] = __builtin_amdgcn_exp2f(sc[0][r] - mx); sc[1][r] = __builtin_amdgcn_exp2f(sc[1][r] - mx); sc[2][r] = __builtin_amdgcn_exp2f(sc[2][r] - mx); sum += sc[0][r] + sc[1][r] + sc[2][r]; }
            sum = xsum32(xsum16(sum));
            const float inv = __builtin_amdgcn_rcpf(sum);
            {
                unsigned a0 = pk2(sc[0][0] * inv, sc[0][1] * inv), a1 = pk2(sc[0][2] * inv, sc[0][3] * inv);
                unsigned b0 = pk2(sc[1][0] * inv, sc[1][1] * inv), b1 = pk2(sc[1][2] * inv, sc[1][3] * inv);
                unsigned c0 = pk2(sc[2][0] * inv, sc[2][1] * inv), c1 = pk2(sc[2][2] * inv, sc[2][3] * inv);
                auto x0 = __builtin_amdgcn_permlane16_swap(b0, c0, false, false); auto x1 = __builtin_amdgcn_permlane16_swap(b1, c1, false, false);
                auto y0 = __builtin_amdgcn_permlane16_swap(a0, a0, false, false); auto y1 = __builtin_amdgcn_permlane16_swap(a1, a1, false, false);
                const unsigned pb = (unsigned)(OFF_P - OFF_MEM) + s * 20480 + h * 640 + l15 * 16;
                if (l15 < 8) {
                    st16c(rs, pb + (1 + (g >> 1) + 2 * (g & 1)) * 128, (v4u){x0[0], x1[0], x0[1], x1[1]});
                    if (g == 0) st16c(rs, pb, (v4u){y0[0], y1[0], y0[1], y1[1]});
                }
            }
        }
        ST(h, s, 3);
        {
            auto x0 = __builtin_amdgcn_permlane16_swap(kp[0].x, kp[1].x, false, false); auto x1 = __builtin_amdgcn_permlane16_swap(kp[0].y, kp[1].y, false, false);
            auto y0 = __builtin_amdgcn_permlane16_swap(kp[2].x, kp[2].x, false, false); auto y1 = __builtin_amdgcn_permlane16_swap(kp[2].y, kp[2].y, false, false);
            const unsigned kb = (unsigned)OFF_KM + (unsigned)((((s * 32 + h) * 8 + l15) * E_ + 48 * w + 8 * (g >> 1)) * 2);
            if (l15 < 8) {
                st16c(rw, kb + 32 * (g & 1), (v4u){x0[0], x1[0], x0[1], x1[1]});
                if ((g & 1) == 0) st16c(rw, kb + 64, (v4u){y0[0], y1[0], y0[1], y1[1]});
            }
        }
        if (s + 1 < NSEG) {
            if (((s + 1) & 3) == 0) wait_cnt(ctl, CW_A + 32 * ((s + 1) >> 2), 68);
#pragma unroll
            for (int kt = 0; kt < 2; ++kt)
#pragma unroll
                for (int j = 0; j < 3; ++j) kx[kt][j] = ld8c(rw, kg0 + (unsigned)(((s + 1) * 32 + 16 * kt) * HE * 2 + 32 * j));
        }
    }
}

__device__ __forceinline__ void chain_vo(char* lds, int h, const u16* __restrict__ Wv, const u16* __restrict__ Wo, char* ws, __amdgpu_buffer_rsrc_t rs) {
    gu32* ctl = (gu32*)(ws + OFF_CTL);
    const int tid = threadIdx.x, lane = tid & 63, w = tid >> 6, g = lane >> 4, l15 = lane & 15;
    const int et0 = 3 * w;
    bf16x8 wv[3][20], wo[10][3];
#pragma unroll
    for (int c = 0; c < 2; ++c) {
        __syncthreads();
        stage192<320, 10>(lds, Wv + (size_t)(c * 320) * HE + h * E_, tid);
        __syncthreads();
#pragma unroll
        for (int k = 0; k < 10; ++k)
#pragma unroll
            for (int j = 0; j < 3; ++j) wv[j][c * 10 + k] = trfrag192(lds, 32 * k, et0 + j, lane);
    }
    const u16* Woh = Wo + (size_t)h * E_ * D_;
    __syncthreads();
    stage640(lds, Woh, tid);
    __syncthreads();
#pragma unroll
    for (int n = 0; n < 10; ++n)
#pragma unroll
        for (int t = 0; t < 3; ++t) wo[n][t] = trfragP<1280>(lds, 32 * t, 10 * w + n, lane);
    __syncthreads();
    stage640(lds + C_RES, Woh + 96 * D_, tid);
    if (tid == 0) lds_flag_set(lds, 0);
    __syncthreads();
    bf16x8 lb[2][5];
#pragma unroll
    for (int n = 0; n < 5; ++n) { lb[0][n] = trfragP<1280>(lds + C_RES, 0, 10 * w + n, lane); lb[1][n] = trfragP<1280>(lds + C_RES, 0, 10 * w + 5 + n, lane); }
    char* vfr = lds + C_SP;
#pragma unroll 1
    for (int s = 0; s < NSEG; ++s) {
        if ((s & 3) == 0) wait_cnt(ctl, CW_A + 32 * (s >> 2), 68);
        ST(32 + h, s, 0);
        poll_mem(rs, s * 10240, lds + C_MEM, tid, ctl);
        bar_lds();
        ST(32 + h, s, 1);
        f32x4 acc[3] = {{0.f, 0.f, 0.f, 0.f}, {0.f, 0.f, 0.f, 0.f}, {0.f, 0.f, 0.f, 0.f}};
        {
            const char* mb = lds + C_MEM + (g * 8 + (lane & 7)) * 16;
            bf16x8 b[20];
#pragma unroll
            for (int p = 0; p < 4; ++p) b[p] = *(const bf16x8*)(mb + p * 512);
#pragma unroll
            for (int p = 0; p < 20; ++p) {
                if (p + 4 < 20) b[p + 4] = *(const bf16x8*)(mb + (p + 4) * 512);
#pragma unroll
                for (int j = 0; j < 3; ++j) acc[j] = mfma16(wv[j][p], b[p], acc[j]);
                SB();
            }
        }
        if (l15 < 8) {
#pragma unroll
            for (int j = 0; j < 3; ++j) *(v2u*)(vfr + ((6 * w + 2 * j + (g >> 1)) * 8 + l15) * 16 + (g & 1) * 8) = (v2u){pk2(acc[j][0], acc[j][1]), pk2(acc[j][2], acc[j][3])};
        }
        bar_lds();
        ST(32 + h, s, 2);
        f32x4 ua[10];
#pragma unroll
        for (int n = 0; n < 10; ++n) ua[n] = (f32x4){0.f, 0.f, 0.f, 0.f};
        {
            const char* vb = vfr + (g * 8 + (lane & 7)) * 16;
            bf16x8 a[6];
#pragma unroll
            for (int t = 0; t < 6; ++t) a[t] = *(const bf16x8*)(vb + t * 512);
#pragma unroll
            for (int k = 0; k < 6; ++k) {
#pragma unroll
                for (int n = 0; n < 5; ++n) ua[5 * (k & 1) + n] = mfma16(a[k >> 1], wo[5 * (k & 1) + n][k >> 1], ua[5 * (k & 1) + n]);
                SB();
#pragma unroll
                for (int n = 0; n < 5; ++n) ua[5 * (k & 1) + n] = mfma16(a[3 + (k >> 1)], lb[k & 1][n], ua[5 * (k & 1) + n]);
                SB();
#pragma unroll
                for (int n = 0; n < 5; ++n) lb[k & 1][n] = trfragP<1280>(lds + C_RES, 32 * (((k + 2) % 6) >> 1), 10 * w + 5 * (k & 1) + n, lane);
                SB();
            }
        }
        {
#pragma unroll
            for (int n = 0; n < 10; n += 2) {
                unsigned a0 = pk2(ua[n][0], ua[n][1]), a1 = pk2(ua[n][2], ua[n][3]), b0 = pk2(ua[n + 1][0], ua[n + 1][1]), b1 = pk2(ua[n + 1][2], ua[n + 1][3]);
                auto x0 = __builtin_amdgcn_permlane16_swap(a0, b0, false, false); auto x1 = __builtin_amdgcn_permlane16_swap(a1, b1, false, false);
                unsigned off = (unsigned)(OFF_UM - OFF_MEM) + ((s * 32 + h) * 40 + 10 * w + n + g) * 256 + l15 * 16;
                if (g < 2) st16c(rs, off, (v4u){x0[0], x1[0], x0[1], x1[1]});
            }
        }
        ST(32 + h, s, 3);
    }
}

__device__ __forceinline__ void chain_red(char* lds, int r, char* ws, __amdgpu_buffer_rsrc_t rs, __amdgpu_buffer_rsrc_t rw) {
    gu32* ctl = (gu32*)(ws + OFF_CTL);
    const int tid = threadIdx.x, lane = tid & 63, w = tid >> 6, g = lane >> 4, l15 = lane & 15;
    float* red = (float*)lds;
    if (tid == 0) lds_flag_set(lds, 0);
    __syncthreads();
    auto ldux = [&](int s, v4u (&ux)[10]) {
#pragma unroll
        for (int t = 0; t < 10; ++t) { int cgl = 4 * t + g, hh = cgl / 5, jg = cgl % 5; int jj = jg ? jg - 1 : 0;
            ux[t] = ld16c(rw, (unsigned)OFF_UX + (unsigned)(((((s * 32 + 8 * w + hh) * 40 + r) * 4 + jj) * 16 + l15) * 16)); }
    };
    v4u ux[10]; wait_cnt(ctl, CW_A, 68); ldux(0, ux);
#pragma unroll 1
    for (int s = 0; s + 1 < NSEG; ++s) {
        v4u pf[10], um0, um1; unsigned spins = 0; bool dead = false;
        ST(64 + r, s, 0);
        const unsigned pb = (unsigned)(OFF_P - OFF_MEM) + s * 20480 + ((40 * w + g) * 8 + (lane & 7)) * 16;
        const unsigned ub = (unsigned)(OFF_UM - OFF_MEM) + ((s * 32 + 8 * w + g) * 40 + r) * 256 + l15 * 16;
        const unsigned pd = (unsigned)(OFF_P - OFF_MEM) + s * 20480 + ((40 * w + 4 * (l15 >> 3) + g) * 8 + (lane & 7)) * 16;
        v4u pr[5];
        while (true) {
            asm volatile("" ::: "memory");
            um0 = ld16c(rs, ub); um1 = ld16c(rs, ub + 4 * 40 * 256);
            if (__all(okv(um0) && okv(um1))) break;
            if (spin_dead(spins, ctl)) { dead = true; break; }
        }
        while (true) {
            asm volatile("" ::: "memory");
#pragma unroll
            for (int t = 0; t < 5; ++t) pr[t] = ld16c(rs, pd + t * 1024);
            bool ok = true;
#pragma unroll
            for (int t = 0; t < 5; ++t) ok &= okv(pr[t]);
            if (__all(ok)) break;
            if (spin_dead(spins, ctl)) { dead = true; break; }
        }
#pragma unroll
        for (int t = 0; t < 5; ++t) {
            pf[2 * t] = pr[t];
            pf[2 * t + 1] = (v4u){(unsigned)__builtin_amdgcn_mov_dpp(pr[t].x, 0x128, 0xf, 0xf, true), (unsigned)__builtin_amdgcn_mov_dpp(pr[t].y, 0x128, 0xf, 0xf, true),
                                  (unsigned)__builtin_amdgcn_mov_dpp(pr[t].z, 0x128, 0xf, 0xf, true), (unsigned)__builtin_amdgcn_mov_dpp(pr[t].w, 0x128, 0xf, 0xf, true)};
        }
        ST(64 + r, s, 1);
        f32x4 acc0 = {0.f, 0.f, 0.f, 0.f}, acc1 = {0.f, 0.f, 0.f, 0.f};
#pragma unroll
        for (int t = 0; t < 5; ++t) {
            v4u a = (t < 4 && g == t) ? um0 : ux[t];
            acc0 = mfma16(__builtin_bit_cast(bf16x8, a), __builtin_bit_cast(bf16x8, pf[t]), acc0);
            v4u b = (t < 4 && g == t) ? um1 : ux[5 + t];
            acc1 = mfma16(__builtin_bit_cast(bf16x8, b), __builtin_bit_cast(bf16x8, pf[5 + t]), acc1);
        }
        acc0 += acc1;
        if (l15 < 8) *(f32x4*)(red + (w * 8 + l15) * 16 + 4 * g) = acc0;
        bar_lds();
        ST(64 + r, s, 2);
        if (w == 0) {
            int i = lane >> 3, cp = lane & 7; float a0 = 0.f, a1 = 0.f;
#pragma unroll
            for (int q = 0; q < 4; ++q) { float2 p = *(const float2*)(red + (q * 8 + i) * 16 + 2 * cp); a0 += p.x; a1 += p.y; }
            unsigned d0 = pk2(a0, a1);
            unsigned d1 = __builtin_amdgcn_mov_dpp(d0, 0x55, 0xf, 0xf, true), d2 = __builtin_amdgcn_mov_dpp(d0, 0xAA, 0xf, 0xf, true), d3 = __builtin_amdgcn_mov_dpp(d0, 0xFF, 0xf, 0xf, true);
            if ((cp & 3) == 0) st16c(rs, (s + 1) * 10240 + r * 256 + (cp >> 2) * 128 + i * 16, (v4u){d0, d1, d2, d3});
        }
        ST(64 + r, s, 3);
        if (s + 2 < NSEG) { if (((s + 1) & 3) == 0) wait_cnt(ctl, CW_A + 32 * ((s + 1) >> 2), 68); ldux(s + 1, ux); }
        bar_lds();
    }
}

struct Params { const u16 *x, *Wq, *Wk, *Wv, *Wo, *w1, *w3, *w2; char* ws; u16* out; };
__global__ void __launch_bounds__(256, 1) k_main(Params p) {
    extern __shared__ __attribute__((aligned(16))) char lds[];
    const int tid = threadIdx.x;
#define MAPM 1
    int b;
    { const int bx = blockIdx.x, xc = bx & 7, sl = bx >> 3;
      if (MAPM == 0) b = bx;
      else if (MAPM == 1) { int id = (xc < 4) ? sl * 4 + xc : 128 + sl * 4 + (xc - 4); b = id; }
      else if (MAPM == 2) { b = (xc == 0) ? sl : (xc == 1) ? 32 + sl : (xc < 4 && sl < 20) ? 64 + (xc - 2) * 20 + sl : -1;
             if (b < 0) { b = 104 + ((xc < 4) ? (xc - 2) * 12 + (sl - 20) : 24 + (xc - 4) * 32 + sl); } }
      else {   b = (xc < 2) ? (sl * 2 + xc) : (xc < 4 && sl < 20) ? 64 + (xc - 2) * 20 + sl : -1;
             if (b < 0) { b = 104 + ((xc < 4) ? (xc - 2) * 12 + (sl - 20) : 24 + (xc - 4) * 32 + sl); } } }
    char* ws = p.ws;
    const __amdgpu_buffer_rsrc_t rs = __builtin_amdgcn_make_buffer_rsrc(ws + OFF_MEM, 0, (int)(OFF_PT - OFF_MEM), 0x00020000);
    const __amdgpu_buffer_rsrc_t rw = __builtin_amdgcn_make_buffer_rsrc(ws, 0, (int)OFF_END, 0x00020000);
    if (b < 32) { chain_qk(lds, b, p.Wq, p.Wk, ws, rs, rw); return; }
    if (b < 64) { chain_vo(lds, b - 32, p.Wv, p.Wo, ws, rs); return; }
    if (b < 104) { chain_red(lds, b - 64, ws, rs, rw); return; }
    gu32* ctl = (gu32*)(ws + OFF_CTL);
    const int* tab = g_tasks.v;
    volatile LAS int* tslot = (volatile LAS int*)(LAS char*)(lds + C_FLAG);
#pragma unroll 1
    while (true) {
        __syncthreads();
        if (tid == 0) { int t = (int)__hip_atomic_fetch_add(ctl + CW_QUEUE, 1u, RLX_AGENT); tslot[0] = t; tslot[1] = t < NTASK_ALL ? tab[t] : -1; }
        __syncthreads();
        const int t = __builtin_amdgcn_readfirstlane(tslot[0]);
        if (t >= NTASK_ALL) break;
        const int code = __builtin_amdgcn_readfirstlane(tslot[1]), ty = code >> 16, ta = (code >> 8) & 255, tb = code & 255;
        int sig = -1;
#define TS1() do {} while (0)
        if (ty == 8) {
            TS1(); taskFill(ta, rw); sig = CW_A + 32 * (ta >> 2);
        } else if (ty == 9) {
            TS1(); taskLN(ta, p.x, ws, rw); sig = CW_LN + 32 * (ta >> 2);
        } else if (ty == 0) {
            if (tid < 64) { wait_cnt(ctl, CW_LN + 32 * ta, 4); if (ta > 0) wait_cnt(ctl, CW_A, 68); }
            __syncthreads();
            TS1(); taskA(lds, ta, tb, 2, p.Wq, p.Wk, p.Wv, p.Wo, ws, rw); sig = CW_A + 32 * ta;
        } else if (ty == 10) {
            if (tid < 64) { wait_cnt(ctl, CW_LN + 32 * ta, 4); if (ta > 0) wait_cnt(ctl, CW_A, 68); }
            __syncthreads();
            TS1(); taskA(lds, ta, tb, 0, p.Wq, p.Wk, p.Wv, p.Wo, ws, rw); sig = CW_A + 32 * ta;
        } else if (ty == 1) {
            if (tid < 64) wait_cnt(ctl, CW_LN + 32 * ta, 4);
            __syncthreads();
            TS1(); taskA(lds, ta, tb, 1, p.Wq, p.Wk, p.Wv, p.Wo, ws, rw); sig = CW_Q + 32 * ta;
        } else if (ty == 2) {
            if (tid < 64) { wait_cnt(ctl, CW_A + 32 * (ta >> 2), 68); wait_cnt(ctl, CW_Q + 32 * (ta >> 2), 32); }
            __syncthreads();
            TS1(); taskP(ta, tb, ws, rw); sig = CW_P + 32 * ta;
        } else if (ty == 3) {
            TS1(); taskO(lds, ta, tb, p.x, ws, rw, ctl); sig = CW_X + 32 * ta;
        } else if (ty == 4) {
            TS1(); taskF1(lds, ta, tb, p.w1, p.w3, ws, rw, ctl); sig = CW_F + 32 * ta;
        } else if (ty == 5) {
            TS1(); taskF2(lds, ta, tb, p.w2, ws, p.out, rw, ctl);
        } else if (ty == 6) {
            if (tid < 64) { for (int j = 0; j < 4; ++j) wait_cnt(ctl, CW_X + 32 * (4 * ta + j), 10); }
            __syncthreads();
            TS1(); taskF1B(lds, ta, tb, p.w1, p.w3, ws, rw); sig = CW_F + 32 * (4 * ta);
        } else {
            if (tid < 64) wait_cnt(ctl, CW_F + 32 * (4 * ta), 16);
            __syncthreads();
            TS1(); taskF2B(lds, ta, tb, p.w2, ws, p.out, rw);
        }
        drain_vm();
        __syncthreads();
        if (tid == 0 && sig >= 0) __hip_atomic_fetch_add(ctl + sig, 1u, RLX_AGENT);
    }
}

extern "C" void kernel_launch(void* const* d_in, const int* in_sizes, int n_in, void* d_out, int out_size, void* d_ws, size_t ws_size, hipStream_t stream) {
    const u16* x = (const u16*)d_in[0]; const u16* mem0 = (const u16*)d_in[1];
    const u16* c = (const u16*)d_in[11]; const u16* wmod = (const u16*)d_in[12];
    char* ws = (char*)d_ws;
    Params p; p.x = x; p.Wq = (const u16*)d_in[2]; p.Wk = (const u16*)d_in[3]; p.Wv = (const u16*)d_in[4]; p.Wo = (const u16*)d_in[5];
    p.w1 = (const u16*)d_in[8]; p.w3 = (const u16*)d_in[9]; p.w2 = (const u16*)d_in[10]; p.ws = ws; p.out = (u16*)d_out;
    (void)hipFuncSetAttribute((const void*)k_main, hipFuncAttributeMaxDynamicSharedMemorySize, C_LDS);
    k_pre<<<121, 256, 0, stream>>>(c, wmod, mem0, ws);
    k_main<<<256, 256, C_LDS, stream>>>(p);
}
```
